```python
import torch

def _softmax(x, axis=-1):
    return torch.softmax(x, dim=axis)
def _gelu(x):
    return torch.nn.functional.gelu(x, approximate="tanh")
def _silu(x):
    return torch.nn.functional.silu(x)
def _sigmoid(x):
    return torch.sigmoid(x)
def _layernorm(x, axis=-1, eps=1e-5):
    m = x.mean(dim=axis, keepdim=True)
    v = x.var(dim=axis, keepdim=True, unbiased=False)
    return (x - m) / torch.sqrt(v + eps)
def _scatter_add(tgt, idx, src, axis=0):
    return tgt.clone().index_add_(axis, idx, src)
def _segment_sum(src, seg, n):
    out = torch.zeros((n,) + src.shape[1:], dtype=src.dtype)
    return out.index_add_(0, seg, src)
def _pad(x, pw):
    flat = tuple(v for pair in reversed(pw) for v in pair)
    return torch.nn.functional.pad(x, flat)

kernel_name = 'standalone__dense_activation_relu__prompt'

def setup_inputs():

    _w = (lambda a: (lambda t: t.to(torch.bfloat16) if t.is_floating_point() else t)(torch.as_tensor(a)))
    B, S, D, Dff = 1, 2048, 1024, 3584
    return {
        "x": _w((lambda *s: torch.randn(*s, dtype=torch.bfloat16) * 0.05)(B, S, D)),
        "w1": _w((lambda *s: torch.randn(*s, dtype=torch.bfloat16) * 0.05)(D, Dff)),
        "w2": _w((lambda *s: torch.randn(*s, dtype=torch.bfloat16) * 0.05)(Dff, D)),
    }

def reference(x, w1, w2):
    h = torch.einsum("bsd,df->bsf", x, w1)
    zero = torch.zeros_like(h)
    h = torch.maximum(h, zero)
    return torch.einsum("bsf,fd->bsd", h, w2)
```

```cpp
#include <hip/hip_runtime.h>
#include <hip/hip_bf16.h>

#define LAS __attribute__((address_space(3)))
#define GAS __attribute__((address_space(1)))
typedef int i32x4 __attribute__((ext_vector_type(4)));
typedef unsigned u32x4 __attribute__((ext_vector_type(4)));
typedef unsigned u32x2 __attribute__((ext_vector_type(2)));
typedef float f32x4 __attribute__((ext_vector_type(4)));
typedef float f32x2 __attribute__((ext_vector_type(2)));
typedef short s16x4 __attribute__((ext_vector_type(4)));
typedef short s16x8 __attribute__((ext_vector_type(8)));
typedef __bf16 bf16x8 __attribute__((ext_vector_type(8)));
typedef __bf16 bf16x2 __attribute__((ext_vector_type(2)));
typedef unsigned long long u64;

namespace {

constexpr int kM = 2048, kD = 1024, kF = 3584;
constexpr unsigned kXBytes = kM * kD * 2, kWBytes = kD * kF * 2, kHBytes = kM * kF * 2, kOBytes = kM * kD * 2;
constexpr size_t kWsH = 0;
constexpr size_t kWsX = 16u << 20;
constexpr unsigned kXchBytes = 256u * 32768u;
constexpr size_t kWsHF = 24u << 20;
constexpr size_t kWsXF = kWsHF + 256 * 128;
constexpr size_t kWsG = kWsXF + 256 * 128;
constexpr unsigned kSlot1 = 49152;
constexpr unsigned kW2Base = 81920;
constexpr int kSpinCap = 1 << 22;

__device__ __forceinline__ i32x4 make_srd(const void* p, unsigned bytes) {
  u64 a = (u64)p;
  i32x4 r;
  r.x = (int)(unsigned)a; r.y = (int)((unsigned)(a >> 32) & 0xffffu); r.z = (int)bytes; r.w = 0x00020000;
  return r;
}
__device__ __forceinline__ void set_m0(unsigned v) { asm volatile("s_mov_b32 m0, %0\n\ts_nop 0" ::"s"(v) : "memory"); }
template <int OFF> __device__ __forceinline__ void dma(i32x4 srd, unsigned voff, unsigned soff) {
  asm volatile("buffer_load_dwordx4 %0, %1, %2 offen offset:%3 lds" ::"v"(voff), "s"(srd), "s"(soff), "n"(OFF) : "memory");
}
template <int OFF> __device__ __forceinline__ void dma_sc1(i32x4 srd, unsigned voff, unsigned soff) {
  asm volatile("buffer_load_dwordx4 %0, %1, %2 offen offset:%3 sc1 lds" ::"v"(voff), "s"(srd), "s"(soff), "n"(OFF) : "memory");
}
template <int N> __device__ __forceinline__ void wait_vm() { asm volatile("s_waitcnt vmcnt(%0)" ::"n"(N) : "memory"); }
__device__ __forceinline__ void wait_lgkm0() { __builtin_amdgcn_s_waitcnt(0xC07F); }
__device__ __forceinline__ void bar() {
  asm volatile("" ::: "memory");
  __builtin_amdgcn_s_barrier();
  asm volatile("" ::: "memory");
}
__device__ __forceinline__ bf16x8 lds_b128(LAS char* base, unsigned off) { return *(LAS bf16x8*)(base + off); }
__device__ __forceinline__ bf16x8 lds_tr2(LAS char* base, unsigned off0, unsigned off1) {
  s16x4 a = __builtin_amdgcn_ds_read_tr16_b64_v4i16((LAS s16x4*)(base + off0));
  s16x4 b = __builtin_amdgcn_ds_read_tr16_b64_v4i16((LAS s16x4*)(base + off1));
  return __builtin_bit_cast(bf16x8, __builtin_shufflevector(a, b, 0, 1, 2, 3, 4, 5, 6, 7));
}
__device__ __forceinline__ unsigned pack_bf16(float lo, float hi) {
  bf16x2 v = __builtin_convertvector((f32x2){lo, hi}, bf16x2);
  return __builtin_bit_cast(unsigned, v);
}
__device__ __forceinline__ float relu(float v) { return __builtin_elementwise_maximum(v, 0.0f); }
#define MFMA(a, b, c) __builtin_amdgcn_mfma_f32_16x16x32_bf16((a), (b), (c), 0, 0, 0)
#define SGB(mask, n) __builtin_amdgcn_sched_group_barrier((mask), (n), 0)

__device__ __forceinline__ u64 ld_agent(const u64* p) { return __hip_atomic_load((GAS u64*)p, __ATOMIC_RELAXED, __HIP_MEMORY_SCOPE_AGENT); }
__device__ __forceinline__ void st_agent(u64* p, u64 v) { __hip_atomic_store((GAS u64*)p, v, __ATOMIC_RELAXED, __HIP_MEMORY_SCOPE_AGENT); }

__global__ __launch_bounds__(512, 2) void mlp_fused(const char* __restrict__ x, const char* __restrict__ w1,
                                                    const char* __restrict__ w2, char* __restrict__ out, char* ws) {
  __shared__ __attribute__((aligned(1024))) char smem_[163840];
  LAS char* smem = (LAS char*)smem_;
  const int wave = __builtin_amdgcn_readfirstlane(threadIdx.x >> 6);
  const unsigned lane = threadIdx.x & 63;
  const int b = blockIdx.x, xcd = b & 7, rr = b >> 3;
  const int bi = (xcd & 1) * 8 + (rr & 7), bj = (xcd >> 1) * 4 + (rr >> 3);
  const int wg = bi * 16 + bj;

  u64 gen;
  asm volatile("global_load_dwordx2 %0, %1, off sc1" : "=v"(gen) : "v"(ws + kWsG + bi * 128) : "memory");

  if (wave >= 4) {
    const int w = wave - 4;
    const i32x4 sx = make_srd(x, kXBytes), sw1 = make_srd(w1, kWBytes), sw2 = make_srd(w2, kWBytes), sh = make_srd(ws + kWsH, kHBytes);
    unsigned vx[4], vw[8];
#pragma unroll
    for (int p = 0; p < 4; ++p)
      vx[p] = (32 * w + 8 * p + (lane >> 3)) * 2048 + (((lane & 7) ^ ((4 * p + (lane >> 4)) & 7)) << 4) - p * 1024;
#pragma unroll
    for (int p = 0; p < 8; ++p) {
      const unsigned row = 16 * w + 2 * p + (lane >> 5), c = lane & 31;
      const unsigned s = (row & 3) | (((row >> 3) & 1) << 2);
      vw[p] = row * 7168 + (((c >> 1) ^ s) << 5) + ((c & 1) << 4) - (p & 3) * 1024;
    }
    const unsigned sx0 = bi * 128 * 2048, sw0 = bj * 448 - (bj & 1) * 64;
    auto issue1 = [&](int t, unsigned slot) {
      const unsigned so_x = sx0 + t * 128, so_w = sw0 + t * (64 * 7168);
      set_m0(slot + w * 4096);
      dma<0>(sx, vx[0], so_x); dma<1024>(sx, vx[1], so_x); dma<2048>(sx, vx[2], so_x); dma<3072>(sx, vx[3], so_x);
      set_m0(slot + 16384 + w * 8192);
      dma<0>(sw1, vw[0], so_w); dma<1024>(sw1, vw[1], so_w); dma<2048>(sw1, vw[2], so_w); dma<3072>(sw1, vw[3], so_w);
      set_m0(slot + 16384 + w * 8192 + 4096);
      dma<0>(sw1, vw[4], so_w); dma<1024>(sw1, vw[5], so_w); dma<2048>(sw1, vw[6], so_w); dma<3072>(sw1, vw[7], so_w);
    };
    issue1(0, 0 * kSlot1); issue1(1, 2 * kSlot1); issue1(2, 1 * kSlot1);
    unsigned v2[2];
#pragma unroll
    for (int e = 0; e < 2; ++e) {
      const unsigned row = 8 * w + 4 * e + (lane >> 4), c = lane & 15;
      v2[e] = row * 2048 + (((c >> 1) ^ (row & 7)) << 5) + ((c & 1) << 4) - e * 1024;
    }
    const unsigned vh = w * 2048 + lane * 16;
    const unsigned w2col = (bj & 7) * 256;
    auto kk_of = [&](int n) -> unsigned {
      if (n < 7) return bj * 7 + n;
      const int u = n - 7, sp = u / 7, d = u % 7 + 1;
      return ((bj & 8) | ((bj + d) & 7)) * 7 + sp;
    };
    auto issue_w2 = [&](int n, unsigned kk) {
      set_m0(kW2Base + ((n + 2) % 10) * 8192 + w * 2048);
      const unsigned so = kk * 65536 + w2col;
      dma<0>(sw2, v2[0], so); dma<1024>(sw2, v2[1], so);
    };
    auto issue_h = [&](int n, unsigned kk) {
      set_m0((n % 10) * 8192 + w * 2048);
      const unsigned so = (bi * 112 + kk) * 8192;
      dma_sc1<0>(sh, vh, so); dma_sc1<1024>(sh, vh, so);
    };
    asm volatile("s_waitcnt vmcnt(24)" : "+v"(gen)::"memory");
    const u64 tag = gen + 1;
    bar();
    unsigned slot = 0;
    for (int t = 0; t < 13; ++t) {
      wait_vm<12>();
      bar();
      issue1(t + 3, slot);
      slot = slot == 0 ? 2 * kSlot1 : slot - kSlot1;
    }
    wait_vm<12>();
    bar();
#pragma unroll
    for (int n = 0; n < 8; ++n) issue_w2(n, kk_of(n));
    wait_vm<16>();
    bar();
    issue_w2(8, kk_of(8)); issue_w2(9, kk_of(9));
    bar();
    wait_vm<0>();
    bar();
    {
      const __amdgpu_buffer_rsrc_t rh = __builtin_amdgcn_make_buffer_rsrc(ws + kWsH, 0, kHBytes, 0x00020000);
#pragma unroll
      for (int s = 0; s < 7; ++s)
#pragma unroll
        for (int e = 0; e < 2; ++e) {
          const u32x4 v = *(LAS u32x4*)(smem + s * 8192 + e * 1024 + vh);
          __builtin_amdgcn_raw_buffer_store_b128(v, rh, vh + e * 1024, (bi * 112 + bj * 7 + s) * 8192, 16);
        }
    }
    wait_lgkm0();
    bar();
    issue_w2(10, kk_of(10)); issue_w2(11, kk_of(11));
    bar();
    issue_w2(12, kk_of(12)); issue_w2(13, kk_of(13));
    wait_vm<8>();
    if (lane == 0) st_agent((u64*)(ws + kWsHF + wg * 128 + w * 8), tag);
    {
      const u64* fp = (const u64*)(ws + kWsHF + (bi * 16 + (bj & 8) + ((lane >> 2) & 7)) * 128 + (lane & 3) * 8);
      for (int spin = 0; spin < kSpinCap; ++spin) {
        const u64 f = ld_agent(fp);
        if (__all(f == tag)) break;
        __builtin_amdgcn_s_sleep(1);
      }
    }
    asm volatile("" ::: "memory");
#pragma unroll
    for (int n = 7; n < 14; ++n) issue_h(n, kk_of(n));
    wait_vm<12>();
    bar();
#pragma unroll
    for (int s = 2; s < 27; ++s) {
      if (s > 2) {
        if (s == 3) wait_vm<16>();
        else if (s == 4) wait_vm<20>();
        else if (s <= 23) wait_vm<24>();
        else if (s == 24) wait_vm<16>();
        else if (s == 25) wait_vm<8>();
        else wait_vm<0>();
        bar();
      }
      if (s <= 22) {
#pragma unroll
        for (int e = 0; e < 2; ++e) {
          const int n = 2 * s + 10 + e;
          const unsigned kk = kk_of(n);
          issue_h(n, kk); issue_w2(n, kk);
        }
      }
    }
    return;
  }

  const int wm = wave >> 1, wn = wave & 1;
  const unsigned r16 = lane & 15, q = lane >> 4, q4 = (lane >> 2) & 3, p4 = lane & 3;
  asm volatile("s_waitcnt vmcnt(0)" : "+v"(gen)::"memory");
  const u64 tag = gen + 1;
  {
    f32x4 acc[4][7];
#pragma unroll
    for (int m = 0; m < 4; ++m)
#pragma unroll
      for (int f = 0; f < 7; ++f) acc[m][f] = (f32x4){0.f, 0.f, 0.f, 0.f};
    unsigned ax[2], aw[7];
#pragma unroll
    for (int ks = 0; ks < 2; ++ks) ax[ks] = (wm * 64 + r16) * 128 + (((4 * ks + q) ^ ((r16 >> 1) & 7)) << 4);
#pragma unroll
    for (int f = 0; f < 7; ++f) {
      const unsigned u = (bj & 1) * 2 + wn * 7 + f, s = q4 | ((q & 1) << 2);
      aw[f] = 16384 + (8 * q + q4) * 512 + ((u ^ s) << 5) + p4 * 8;
    }
    bf16x8 xf[2][4], wf[2][7];
    auto rd1 = [&](int buf, unsigned slot, int ks) {
#pragma unroll
      for (int m = 0; m < 4; ++m) xf[buf][m] = lds_b128(smem, slot + ax[ks] + m * 2048);
#pragma unroll
      for (int f = 0; f < 7; ++f) wf[buf][f] = lds_tr2(smem, slot + aw[f] + ks * 16384, slot + aw[f] + ks * 16384 + 2048);
    };
    auto mm1 = [&](int buf) {
#pragma unroll
      for (int f = 0; f < 7; ++f)
#pragma unroll
        for (int m = 0; m < 4; ++m) acc[m][f] = MFMA(wf[buf][f], xf[buf][m], acc[m][f]);
    };
    auto sched1 = [&]() {
#pragma unroll
      for (int k = 0; k < 18; ++k) { SGB(0x008, 1); SGB(0x100, 1); }
      SGB(0x008, 10);
    };
    bar();
    unsigned slot = 0;
    rd1(0, slot, 0);
    for (int t = 0; t < 15; ++t) {
      rd1(1, slot, 1);
      mm1(0);
      sched1();
      __builtin_amdgcn_sched_barrier(0);
      wait_lgkm0();
      bar();
      slot = slot == 0 ? 2 * kSlot1 : slot - kSlot1;
      rd1(0, slot, 0);
      mm1(1);
      sched1();
      __builtin_amdgcn_sched_barrier(0);
    }
    rd1(1, slot, 1);
    mm1(0);
    sched1();
    __builtin_amdgcn_sched_barrier(0);
    wait_lgkm0();
    bar();
    mm1(1);
#pragma unroll
    for (int f = 0; f < 7; ++f) {
      const unsigned fb = wn * 7 + f;
#pragma unroll
      for (int m = 0; m < 4; ++m) {
        u32x2 v;
        v.x = pack_bf16(relu(acc[m][f][0]), relu(acc[m][f][1]));
        v.y = pack_bf16(relu(acc[m][f][2]), relu(acc[m][f][3]));
        *(LAS u32x2*)(smem + (fb >> 1) * 8192 + (wm * 4 + m) * 1024 + lane * 16 + (fb & 1) * 8) = v;
      }
    }
    wait_lgkm0();
    bar();
  }
  f32x4 acc[4][4];
#pragma unroll
  for (int m = 0; m < 4; ++m)
#pragma unroll
    for (int n = 0; n < 4; ++n) acc[m][n] = (f32x4){0.f, 0.f, 0.f, 0.f};
  {
    const unsigned ah = wm * 4096 + lane * 16;
    unsigned a2[4];
#pragma unroll
    for (int n = 0; n < 4; ++n) a2[n] = kW2Base + (4 * q + q4) * 256 + (((wn * 4 + n) ^ ((4 * q + q4) & 7)) << 5) + p4 * 8;
    bf16x8 hf[2][4], wf[2][4];
    auto rd2 = [&](int buf, unsigned hs, unsigned wsl) {
#pragma unroll
      for (int m = 0; m < 4; ++m) hf[buf][m] = lds_b128(smem, hs + ah + m * 1024);
#pragma unroll
      for (int n = 0; n < 4; ++n) wf[buf][n] = lds_tr2(smem, wsl + a2[n], wsl + a2[n] + 4096);
    };
    auto mm2 = [&](int buf) {
#pragma unroll
      for (int n = 0; n < 4; ++n)
#pragma unroll
        for (int m = 0; m < 4; ++m) acc[m][n] = MFMA(wf[buf][n], hf[buf][m], acc[m][n]);
    };
    auto sched2 = [&]() {
#pragma unroll
      for (int k = 0; k < 6; ++k) { SGB(0x008, 1); SGB(0x100, 2); }
      SGB(0x008, 10);
    };
    unsigned hs = 0, wsl = 2 * 8192;
    rd2(0, hs, wsl);
    for (int s = 0; s < 27; ++s) {
      rd2(1, hs + 8192, wsl + 8192);
      mm2(0);
      sched2();
      __builtin_amdgcn_sched_barrier(0);
      wait_lgkm0();
      bar();
      hs = hs == 8 * 8192 ? 0 : hs + 2 * 8192;
      wsl = wsl == 8 * 8192 ? 0 : wsl + 2 * 8192;
      rd2(0, hs, wsl);
      mm2(1);
      sched2();
      __builtin_amdgcn_sched_barrier(0);
    }
    rd2(1, hs + 8192, wsl + 8192);
    mm2(0);
    mm2(1);
  }
  {
    const int kh = bj >> 3, pw = wg ^ 8;
    const __amdgpu_buffer_rsrc_t rx = __builtin_amdgcn_make_buffer_rsrc(ws + kWsX, 0, kXchBytes, 0x00020000);
    f32x4 keep[2][4];
#pragma unroll
    for (int e = 0; e < 2; ++e)
#pragma unroll
      for (int n = 0; n < 4; ++n) {
        const f32x4 snd = kh ? acc[e][n] : acc[2 + e][n];
        keep[e][n] = kh ? acc[2 + e][n] : acc[e][n];
        __builtin_amdgcn_raw_buffer_store_b128(__builtin_bit_cast(u32x4, snd), rx, lane * 16 + (e * 4 + n) * 1024, pw * 32768 + wave * 8192, 16);
      }
    wait_vm<0>();
    if (lane == 0) st_agent((u64*)(ws + kWsXF + pw * 128 + wave * 8), tag);
    const u64* fp = (const u64*)(ws + kWsXF + wg * 128 + wave * 8);
    for (int spin = 0; spin < kSpinCap; ++spin) {
      const u64 f = ld_agent(fp);
      if (__all(f == tag)) break;
      __builtin_amdgcn_s_sleep(1);
    }
    asm volatile("" ::: "memory");
    const __amdgpu_buffer_rsrc_t ro = __builtin_amdgcn_make_buffer_rsrc(out, 0, kOBytes, 0x00020000);
    u32x4 rcv[2][4];
#pragma unroll
    for (int e = 0; e < 2; ++e)
#pragma unroll
      for (int n = 0; n < 4; ++n)
        rcv[e][n] = __builtin_amdgcn_raw_buffer_load_b128(rx, lane * 16 + (e * 4 + n) * 1024, wg * 32768 + wave * 8192, 16);
    const unsigned obase = (bi * 128 + wm * 64 + kh * 32) * 2048 + ((bj & 7) * 128 + wn * 64) * 2;
#pragma unroll
    for (int e = 0; e < 2; ++e)
#pragma unroll
      for (int n = 0; n < 4; ++n) {
        const f32x4 o = keep[e][n] + __builtin_bit_cast(f32x4, rcv[e][n]);
        u32x2 v;
        v.x = pack_bf16(o[0], o[1]); v.y = pack_bf16(o[2], o[3]);
        __builtin_amdgcn_raw_buffer_store_b64(v, ro, (e * 16 + r16) * 2048 + n * 32 + q * 8, obase, 0);
      }
    if (bj == 0 && wave == 0 && lane == 0) st_agent((u64*)(ws + kWsG + bi * 128), tag);
  }
}

}

extern "C" void kernel_launch(void* const* d_in, const int* in_sizes, int n_in, void* d_out, int out_size, void* d_ws,
                              size_t ws_size, hipStream_t stream) {
  (void)in_sizes; (void)n_in; (void)out_size; (void)ws_size;
  mlp_fused<<<256, 512, 0, stream>>>((const char*)d_in[0], (const char*)d_in[1], (const char*)d_in[2], (char*)d_out, (char*)d_ws);
}
```

```cpp
#define ROT 4
#define PF2 1
#define PFT (4 + (bi & 7))
#define SCHED2 1
#include <hip/hip_runtime.h>
#include <hip/hip_bf16.h>

#define LAS __attribute__((address_space(3)))
#define GAS __attribute__((address_space(1)))
typedef int i32x4 __attribute__((ext_vector_type(4)));
typedef unsigned u32x4 __attribute__((ext_vector_type(4)));
typedef unsigned u32x2 __attribute__((ext_vector_type(2)));
typedef float f32x4 __attribute__((ext_vector_type(4)));
typedef float f32x2 __attribute__((ext_vector_type(2)));
typedef short s16x4 __attribute__((ext_vector_type(4)));
typedef short s16x8 __attribute__((ext_vector_type(8)));
typedef __bf16 bf16x8 __attribute__((ext_vector_type(8)));
typedef __bf16 bf16x2 __attribute__((ext_vector_type(2)));
typedef unsigned long long u64;

namespace {

constexpr int kM = 2048, kD = 1024, kF = 3584;
constexpr unsigned kXBytes = kM * kD * 2, kWBytes = kD * kF * 2, kHBytes = kM * kF * 2, kOBytes = kM * kD * 2;
constexpr size_t kWsH = 0;
constexpr size_t kWsX = 16u << 20;
constexpr unsigned kXchBytes = 256u * 32768u;
constexpr size_t kWsHF = 24u << 20;
constexpr size_t kWsXF = kWsHF + 256 * 128;
constexpr size_t kWsG = kWsXF + 256 * 128;
constexpr unsigned kSlot1 = 49152;
constexpr unsigned kW2Base = 81920;
constexpr int kSpinCap = 1 << 22;
#define STAMP(role, k)
#define STAMP_DECL
#define STAMP_FLUSH(role)

__device__ __forceinline__ i32x4 make_srd(const void* p, unsigned bytes) {
  u64 a = (u64)p;
  i32x4 r;
  r.x = (int)(unsigned)a; r.y = (int)((unsigned)(a >> 32) & 0xffffu); r.z = (int)bytes; r.w = 0x00020000;
  return r;
}
__device__ __forceinline__ void set_m0(unsigned v) { asm volatile("s_mov_b32 m0, %0\n\ts_nop 0" ::"s"(v) : "memory"); }
template <int OFF> __device__ __forceinline__ void dma(i32x4 srd, unsigned voff, unsigned soff) {
  asm volatile("buffer_load_dwordx4 %0, %1, %2 offen offset:%3 lds" ::"v"(voff), "s"(srd), "s"(soff), "n"(OFF) : "memory");
}
template <int OFF> __device__ __forceinline__ void dma_sc1(i32x4 srd, unsigned voff, unsigned soff) {
  asm volatile("buffer_load_dwordx4 %0, %1, %2 offen offset:%3 sc1 lds" ::"v"(voff), "s"(srd), "s"(soff), "n"(OFF) : "memory");
}
template <int N> __device__ __forceinline__ void wait_vm() { asm volatile("s_waitcnt vmcnt(%0)" ::"n"(N) : "memory"); }
__device__ __forceinline__ void wait_lgkm0() { __builtin_amdgcn_s_waitcnt(0xC07F); }
__device__ __forceinline__ void bar() {
  asm volatile("" ::: "memory");
  __builtin_amdgcn_s_barrier();
  asm volatile("" ::: "memory");
}
__device__ __forceinline__ bf16x8 lds_b128(LAS char* base, unsigned off) { return *(LAS bf16x8*)(base + off); }
__device__ __forceinline__ bf16x8 lds_tr2(LAS char* base, unsigned off0, unsigned off1) {
  s16x4 a = __builtin_amdgcn_ds_read_tr16_b64_v4i16((LAS s16x4*)(base + off0));
  s16x4 b = __builtin_amdgcn_ds_read_tr16_b64_v4i16((LAS s16x4*)(base + off1));
  return __builtin_bit_cast(bf16x8, __builtin_shufflevector(a, b, 0, 1, 2, 3, 4, 5, 6, 7));
}
__device__ __forceinline__ unsigned pack_bf16(float lo, float hi) {
  bf16x2 v = __builtin_convertvector((f32x2){lo, hi}, bf16x2);
  return __builtin_bit_cast(unsigned, v);
}
__device__ __forceinline__ float relu(float v) { return __builtin_elementwise_maximum(v, 0.0f); }
#define MFMA(a, b, c) __builtin_amdgcn_mfma_f32_16x16x32_bf16((a), (b), (c), 0, 0, 0)
#define SGB(mask, n) __builtin_amdgcn_sched_group_barrier((mask), (n), 0)

__device__ __forceinline__ u64 ld_agent(const u64* p) { return __hip_atomic_load((GAS u64*)p, __ATOMIC_RELAXED, __HIP_MEMORY_SCOPE_AGENT); }
__device__ __forceinline__ void st_agent(u64* p, u64 v) { __hip_atomic_store((GAS u64*)p, v, __ATOMIC_RELAXED, __HIP_MEMORY_SCOPE_AGENT); }

__global__ __launch_bounds__(512, 2) void mlp_fused(const char* __restrict__ x, const char* __restrict__ w1,
                                                    const char* __restrict__ w2, char* __restrict__ out, char* ws) {
  __shared__ __attribute__((aligned(1024))) char smem_[163840];
  LAS char* smem = (LAS char*)smem_;
  const int wave = __builtin_amdgcn_readfirstlane(threadIdx.x >> 6);
  const unsigned lane = threadIdx.x & 63;
  const int b = blockIdx.x, xcd = b & 7, rr = b >> 3;
  const int bi = (xcd & 1) * 8 + (rr & 7), bj = (xcd >> 1) * 4 + (rr >> 3);
  const int wg = bi * 16 + bj;

  STAMP_DECL
  STAMP(0, 0);
  auto load_tag = [&]() -> u64 {
    u64 g;
    asm volatile("s_load_dwordx2 %0, %1, 0x0\n\ts_waitcnt lgkmcnt(0)" : "=s"(g) : "s"(ws + kWsG + bi * 128) : "memory");
    return g + 1;
  };

  if (wave >= 4) {
    const int w = wave - 4;
    const i32x4 sx = make_srd(x, kXBytes), sw1 = make_srd(w1, kWBytes), sw2 = make_srd(w2, kWBytes), sh = make_srd(ws + kWsH, kHBytes);
    unsigned vx[4], vw[8];
#pragma unroll
    for (int p = 0; p < 4; ++p)
      vx[p] = (32 * w + 8 * p + (lane >> 3)) * 2048 + (((lane & 7) ^ ((4 * p + (lane >> 4)) & 7)) << 4) - p * 1024;
#pragma unroll
    for (int p = 0; p < 8; ++p) {
      const unsigned row = 16 * w + 2 * p + (lane >> 5), c = lane & 31;
      const unsigned s = (row & 3) | (((row >> 3) & 1) << 2);
      vw[p] = row * 7168 + (((c >> 1) ^ s) << 5) + ((c & 1) << 4) - (p & 3) * 1024;
    }
    const unsigned sx0 = bi * 128 * 2048, sw0 = bj * 448 - (bj & 1) * 64;
    const int rot = 4 * ((bi >> 2) + (bj >> 2));
    auto issue1 = [&](int t0, unsigned slot) {
      const int t = (t0 + rot) & 15;
      const unsigned so_x = sx0 + t * 128, so_w = sw0 + t * (64 * 7168);
      set_m0(slot + w * 4096);
      dma<0>(sx, vx[0], so_x); dma<1024>(sx, vx[1], so_x); dma<2048>(sx, vx[2], so_x); dma<3072>(sx, vx[3], so_x);
      set_m0(slot + 16384 + w * 8192);
      dma<0>(sw1, vw[0], so_w); dma<1024>(sw1, vw[1], so_w); dma<2048>(sw1, vw[2], so_w); dma<3072>(sw1, vw[3], so_w);
      set_m0(slot + 16384 + w * 8192 + 4096);
      dma<0>(sw1, vw[4], so_w); dma<1024>(sw1, vw[5], so_w); dma<2048>(sw1, vw[6], so_w); dma<3072>(sw1, vw[7], so_w);
    };
    issue1(0, 0 * kSlot1); issue1(1, 2 * kSlot1); issue1(2, 1 * kSlot1);
    unsigned v2[2];
#pragma unroll
    for (int e = 0; e < 2; ++e) {
      const unsigned row = 8 * w + 4 * e + (lane >> 4), c = lane & 15;
      v2[e] = row * 2048 + (((c >> 1) ^ (row & 7)) << 5) + ((c & 1) << 4) - e * 1024;
    }
    const unsigned vh = w * 2048 + lane * 16;
    const unsigned w2col = (bj & 7) * 256;
    auto kk_of = [&](int n) -> unsigned {
      if (n < 7) return bj * 7 + n;
      const int u = n - 7, sp = u / 7, d = u % 7 + 1;
      return ((bj & 8) | ((bj + d) & 7)) * 7 + sp;
    };
    auto issue_w2 = [&](int n, unsigned kk) {
      set_m0(kW2Base + ((n + 2) % 10) * 8192 + w * 2048);
      const unsigned so = kk * 65536 + w2col;
      dma<0>(sw2, v2[0], so); dma<1024>(sw2, v2[1], so);
    };
    auto issue_h = [&](int n, unsigned kk) {
      set_m0((n % 10) * 8192 + w * 2048);
      const unsigned so = (bi * 112 + kk) * 8192;
      dma_sc1<0>(sh, vh, so); dma_sc1<1024>(sh, vh, so);
    };
    const u64 tag = load_tag();
    wait_vm<24>();
    bar();
    STAMP(1, 1);
    unsigned slot = 0;
    for (int t = 0; t < 13; ++t) {
      wait_vm<12>();
      bar();
      issue1(t + 3, slot);
      if (t == PFT) {
        unsigned idx = w * 64 + lane; idx = idx < 224 ? idx : 223;
        set_m0(147456 + w * 256);
        asm volatile("buffer_load_dword %0, %1, %2 offen lds" ::"v"((idx >> 1) * 2048 + (idx & 1) * 128), "s"(sw2), "s"(((bj >> 3) * 1792 + bi * 112) * 2048 + w2col) : "memory");
      }
      slot = slot == 0 ? 2 * kSlot1 : slot - kSlot1;
    }
    wait_vm<12>();
    bar();
    STAMP(1, 2);
#pragma unroll
    for (int n = 0; n < 8; ++n) issue_w2(n, kk_of(n));
    wait_vm<16>();
    bar();
    issue_w2(8, kk_of(8)); issue_w2(9, kk_of(9));
    bar();
    wait_vm<0>();
    bar();
    STAMP(1, 3);
    {
      const __amdgpu_buffer_rsrc_t rh = __builtin_amdgcn_make_buffer_rsrc(ws + kWsH, 0, kHBytes, 0x00020000);
#pragma unroll
      for (int s = 0; s < 7; ++s)
#pragma unroll
        for (int e = 0; e < 2; ++e) {
          const u32x4 v = *(LAS u32x4*)(smem + s * 8192 + e * 1024 + vh);
          __builtin_amdgcn_raw_buffer_store_b128(v, rh, vh + e * 1024, (bi * 112 + bj * 7 + s) * 8192, 16);
        }
    }
    wait_lgkm0();
    bar();
    STAMP(1, 4);
    issue_w2(10, kk_of(10)); issue_w2(11, kk_of(11));
    bar();
    issue_w2(12, kk_of(12)); issue_w2(13, kk_of(13));
    wait_vm<8>();
    if (lane == 0) st_agent((u64*)(ws + kWsHF + wg * 128 + w * 8), tag);
    {
      const u64* fp = (const u64*)(ws + kWsHF + (bi * 16 + (bj & 8) + ((lane >> 2) & 7)) * 128 + (lane & 3) * 8);
      STAMP(1, 5);
      for (int spin = 0; spin < kSpinCap; ++spin) {
        const u64 f = ld_agent(fp);
        if (__all(f == tag)) break;
        __builtin_amdgcn_s_sleep(1);
      }
    }
    asm volatile("" ::: "memory");
    STAMP(1, 6);
#pragma unroll
    for (int n = 7; n < 14; ++n) issue_h(n, kk_of(n));
    wait_vm<12>();
    bar();
    STAMP(1, 7);
#pragma unroll
    for (int s = 2; s < 27; ++s) {
      if (s > 2) {
        if (s == 3) wait_vm<16>();
        else if (s == 4) wait_vm<20>();
        else if (s <= 23) wait_vm<24>();
        else if (s == 24) wait_vm<16>();
        else if (s == 25) wait_vm<8>();
        else wait_vm<0>();
        bar();
      }
      if (s <= 22) {
#pragma unroll
        for (int e = 0; e < 2; ++e) {
          const int n = 2 * s + 10 + e;
          const unsigned kk = kk_of(n);
          issue_h(n, kk); issue_w2(n, kk);
        }
      }
    }
    STAMP(1, 8);
    STAMP_FLUSH(1);
    return;
  }

  const int wm = wave >> 1, wn = wave & 1;
  const unsigned r16 = lane & 15, q = lane >> 4, q4 = (lane >> 2) & 3, p4 = lane & 3;
  const u64 tag = load_tag();
  {
    f32x4 acc[4][7];
#pragma unroll
    for (int m = 0; m < 4; ++m)
#pragma unroll
      for (int f = 0; f < 7; ++f) acc[m][f] = (f32x4){0.f, 0.f, 0.f, 0.f};
    unsigned ax[2], aw[7];
#pragma unroll
    for (int ks = 0; ks < 2; ++ks) ax[ks] = (wm * 64 + r16) * 128 + (((4 * ks + q) ^ ((r16 >> 1) & 7)) << 4);
#pragma unroll
    for (int f = 0; f < 7; ++f) {
      const unsigned u = (bj & 1) * 2 + wn * 7 + f, s = q4 | ((q & 1) << 2);
      aw[f] = 16384 + (8 * q + q4) * 512 + ((u ^ s) << 5) + p4 * 8;
    }
    bf16x8 xf[2][4], wf[2][7];
    auto rd1 = [&](int buf, unsigned slot, int ks) {
#pragma unroll
      for (int m = 0; m < 4; ++m) xf[buf][m] = lds_b128(smem, slot + ax[ks] + m * 2048);
#pragma unroll
      for (int f = 0; f < 7; ++f) wf[buf][f] = lds_tr2(smem, slot + aw[f] + ks * 16384, slot + aw[f] + ks * 16384 + 2048);
    };
    auto mm1 = [&](int buf) {
#pragma unroll
      for (int f = 0; f < 7; ++f)
#pragma unroll
        for (int m = 0; m < 4; ++m) acc[m][f] = MFMA(wf[buf][f], xf[buf][m], acc[m][f]);
    };
    auto sched1 = [&]() {
#pragma unroll
      for (int k = 0; k < 18; ++k) { SGB(0x008, 1); SGB(0x100, 1); }
      SGB(0x008, 10);
    };
    bar();
    STAMP(0, 1);
    unsigned slot = 0;
    rd1(0, slot, 0);
    for (int t = 0; t < 15; ++t) {
      rd1(1, slot, 1);
      mm1(0);
      sched1();
      __builtin_amdgcn_sched_barrier(0);
      wait_lgkm0();
      bar();
      slot = slot == 0 ? 2 * kSlot1 : slot - kSlot1;
      rd1(0, slot, 0);
      mm1(1);
      sched1();
      __builtin_amdgcn_sched_barrier(0);
    }
    rd1(1, slot, 1);
    mm1(0);
    sched1();
    __builtin_amdgcn_sched_barrier(0);
    wait_lgkm0();
    bar();
    STAMP(0, 2);
    mm1(1);
#pragma unroll
    for (int f = 0; f < 7; ++f) {
      const unsigned fb = wn * 7 + f;
#pragma unroll
      for (int m = 0; m < 4; ++m) {
        u32x2 v;
        v.x = pack_bf16(relu(acc[m][f][0]), relu(acc[m][f][1]));
        v.y = pack_bf16(relu(acc[m][f][2]), relu(acc[m][f][3]));
        *(LAS u32x2*)(smem + (fb >> 1) * 8192 + (wm * 4 + m) * 1024 + lane * 16 + (fb & 1) * 8) = v;
      }
    }
    wait_lgkm0();
    bar();
    STAMP(0, 3);
  }
  f32x4 acc[4][4];
#pragma unroll
  for (int m = 0; m < 4; ++m)
#pragma unroll
    for (int n = 0; n < 4; ++n) acc[m][n] = (f32x4){0.f, 0.f, 0.f, 0.f};
  {
    const unsigned ah = wm * 4096 + lane * 16;
    unsigned a2[4];
#pragma unroll
    for (int n = 0; n < 4; ++n) a2[n] = kW2Base + (4 * q + q4) * 256 + (((wn * 4 + n) ^ ((4 * q + q4) & 7)) << 5) + p4 * 8;
    bf16x8 hf[2][4], wf[2][4];
    auto rd2 = [&](int buf, unsigned hs, unsigned wsl) {
#pragma unroll
      for (int m = 0; m < 4; ++m) hf[buf][m] = lds_b128(smem, hs + ah + m * 1024);
#pragma unroll
      for (int n = 0; n < 4; ++n) wf[buf][n] = lds_tr2(smem, wsl + a2[n], wsl + a2[n] + 4096);
    };
    auto mm2 = [&](int buf) {
#pragma unroll
      for (int n = 0; n < 4; ++n)
#pragma unroll
        for (int m = 0; m < 4; ++m) acc[m][n] = MFMA(wf[buf][n], hf[buf][m], acc[m][n]);
    };
    auto sched2 = [&]() {
#pragma unroll
      for (int k = 0; k < 12; ++k) { SGB(0x008, 1); SGB(0x100, 1); }
      SGB(0x008, 4);
    };
    unsigned hs = 0, wsl = 2 * 8192;
    rd2(0, hs, wsl);
    for (int s = 0; s < 27; ++s) {
      rd2(1, hs + 8192, wsl + 8192);
      mm2(0);
      sched2();
      __builtin_amdgcn_sched_barrier(0);
      wait_lgkm0();
      bar();
      hs = hs == 8 * 8192 ? 0 : hs + 2 * 8192;
      wsl = wsl == 8 * 8192 ? 0 : wsl + 2 * 8192;
      rd2(0, hs, wsl);
      mm2(1);
      sched2();
      __builtin_amdgcn_sched_barrier(0);
    }
    rd2(1, hs + 8192, wsl + 8192);
    mm2(0);
    mm2(1);
  }
  STAMP(0, 4);
  {
    const int kh = bj >> 3, pw = wg ^ 8;
    const __amdgpu_buffer_rsrc_t rx = __builtin_amdgcn_make_buffer_rsrc(ws + kWsX, 0, kXchBytes, 0x00020000);
    f32x4 keep[2][4];
#pragma unroll
    for (int e = 0; e < 2; ++e)
#pragma unroll
      for (int n = 0; n < 4; ++n) {
        const f32x4 snd = kh ? acc[e][n] : acc[2 + e][n];
        keep[e][n] = kh ? acc[2 + e][n] : acc[e][n];
        __builtin_amdgcn_raw_buffer_store_b128(__builtin_bit_cast(u32x4, snd), rx, lane * 16 + (e * 4 + n) * 1024, pw * 32768 + wave * 8192, 16);
      }
    wait_vm<0>();
    if (lane == 0) st_agent((u64*)(ws + kWsXF + pw * 128 + wave * 8), tag);
    const u64* fp = (const u64*)(ws + kWsXF + wg * 128 + wave * 8);
    STAMP(0, 5);
    for (int spin = 0; spin < kSpinCap; ++spin) {
      const u64 f = ld_agent(fp);
      if (__all(f == tag)) break;
      __builtin_amdgcn_s_sleep(1);
    }
    asm volatile("" ::: "memory");
    STAMP(0, 6);
    const __amdgpu_buffer_rsrc_t ro = __builtin_amdgcn_make_buffer_rsrc(out, 0, kOBytes, 0x00020000);
    u32x4 rcv[2][4];
#pragma unroll
    for (int e = 0; e < 2; ++e)
#pragma unroll
      for (int n = 0; n < 4; ++n)
        rcv[e][n] = __builtin_amdgcn_raw_buffer_load_b128(rx, lane * 16 + (e * 4 + n) * 1024, wg * 32768 + wave * 8192, 16);
    const unsigned obase = (bi * 128 + wm * 64 + kh * 32) * 2048 + ((bj & 7) * 128 + wn * 64) * 2;
#pragma unroll
    for (int e = 0; e < 2; ++e)
#pragma unroll
      for (int n = 0; n < 4; ++n) {
        const f32x4 o = keep[e][n] + __builtin_bit_cast(f32x4, rcv[e][n]);
        u32x2 v;
        v.x = pack_bf16(o[0], o[1]); v.y = pack_bf16(o[2], o[3]);
        __builtin_amdgcn_raw_buffer_store_b64(v, ro, (e * 16 + r16) * 2048 + n * 32 + q * 8, obase, 0);
      }
    STAMP(0, 7);
    STAMP_FLUSH(0);
    if (bj == 0 && wave == 0 && lane == 0) st_agent((u64*)(ws + kWsG + bi * 128), tag);
  }
}

}

extern "C" void kernel_launch(void* const* d_in, const int* in_sizes, int n_in, void* d_out, int out_size, void* d_ws,
                              size_t ws_size, hipStream_t stream) {
  (void)in_sizes; (void)n_in; (void)out_size; (void)ws_size;
  mlp_fused<<<256, 512, 0, stream>>>((const char*)d_in[0], (const char*)d_in[1], (const char*)d_in[2], (char*)d_out, (char*)d_ws);
}
```

```cpp
#define OWNX 0
#define FL3 0
#define XBF 1
#define PRIO 3
#define PRIOL 1
#define TL2 0
#define XRND 1
#define EARLY 2
#define WF 2
#define CPL 0
#define TSL 0
#define ROT 4
#define PF2 1
#define PFT (4 + (bi & 7))
#define OAUX 2
#define CPF 1
#define LFIRST 1
#define TCH 4
#define PA 4
#define PB 8
#define PC 0
#define PC0 2
#define PC1 5
#define XST 128
#define DLV1 0
#define W13 0
#define B0 0
#define B1 3
#define BSL 2
#define PKS 4
#define POLL2 0
#include <hip/hip_runtime.h>
#include <hip/hip_bf16.h>
#include <type_traits>

#define LAS __attribute__((address_space(3)))
#define GAS __attribute__((address_space(1)))
typedef int i32x4 __attribute__((ext_vector_type(4)));
typedef unsigned u32x4 __attribute__((ext_vector_type(4)));
typedef unsigned u32x2 __attribute__((ext_vector_type(2)));
typedef float f32x4 __attribute__((ext_vector_type(4)));
typedef float f32x2 __attribute__((ext_vector_type(2)));
typedef short s16x4 __attribute__((ext_vector_type(4)));
typedef short s16x8 __attribute__((ext_vector_type(8)));
typedef __bf16 bf16x8 __attribute__((ext_vector_type(8)));
typedef __bf16 bf16x2 __attribute__((ext_vector_type(2)));
typedef unsigned long long u64;
typedef float f32x32 __attribute__((ext_vector_type(32)));

namespace {

constexpr int kM = 2048, kD = 1024, kF = 3584;
constexpr unsigned kXBytes = kM * kD * 2, kWBytes = kD * kF * 2, kHBytes = kM * kF * 2, kOBytes = kM * kD * 2;
constexpr size_t kWsH = 0;
constexpr size_t kWsX = 16u << 20;
constexpr unsigned kXchBytes = 256u * 32768u;
constexpr size_t kWsHF = 24u << 20;
constexpr size_t kWsXF = kWsHF + 256 * 256;
constexpr size_t kWsG = kWsXF + 256 * 512;
constexpr unsigned kSlot1 = 49152;
constexpr unsigned kW2Base = 81920;
constexpr int kSpinCap = 1 << 22;
#define STAMP(k)
#define PS(k)
#define STAMP_DECL
#define STAMP_FLUSH()

#define SA(k) STAMP(k)
#define SB(k)
__device__ __forceinline__ i32x4 make_srd(const void* p, unsigned bytes) {
  u64 a = (u64)p;
  i32x4 r;
  r.x = (int)(unsigned)a; r.y = (int)((unsigned)(a >> 32) & 0xffffu); r.z = (int)bytes; r.w = 0x00020000;
  return r;
}
__device__ __forceinline__ void set_m0(unsigned v) { asm volatile("s_mov_b32 m0, %0\n\ts_nop 0" ::"s"(v) : "memory"); }
template <int OFF> __device__ __forceinline__ void dma(i32x4 srd, unsigned voff, unsigned soff) {
  asm volatile("buffer_load_dwordx4 %0, %1, %2 offen offset:%3 lds" ::"v"(voff), "s"(srd), "s"(soff), "n"(OFF) : "memory");
}
template <int OFF> __device__ __forceinline__ void dma_sc1(i32x4 srd, unsigned voff, unsigned soff) {
  asm volatile("buffer_load_dwordx4 %0, %1, %2 offen offset:%3 sc1 lds" ::"v"(voff), "s"(srd), "s"(soff), "n"(OFF) : "memory");
}
template <int N> __device__ __forceinline__ void wait_vm() { asm volatile("s_waitcnt vmcnt(%0)" ::"n"(N) : "memory"); }
__device__ __forceinline__ void wait_lgkm0() { __builtin_amdgcn_s_waitcnt(0xC07F); }
__device__ __forceinline__ void bar() {
  asm volatile("" ::: "memory");
  __builtin_amdgcn_s_barrier();
  asm volatile("" ::: "memory");
}
__device__ __forceinline__ void barG() {
  bar();
}
__device__ __forceinline__ bf16x8 lds_b128(LAS char* base, unsigned off) { return *(LAS bf16x8*)(base + off); }
__device__ __forceinline__ bf16x8 lds_tr2(LAS char* base, unsigned off0, unsigned off1) {
  s16x4 a = __builtin_amdgcn_ds_read_tr16_b64_v4i16((LAS s16x4*)(base + off0));
  s16x4 b = __builtin_amdgcn_ds_read_tr16_b64_v4i16((LAS s16x4*)(base + off1));
  return __builtin_bit_cast(bf16x8, __builtin_shufflevector(a, b, 0, 1, 2, 3, 4, 5, 6, 7));
}
__device__ __forceinline__ unsigned pack_bf16(float lo, float hi) {
  bf16x2 v = __builtin_convertvector((f32x2){lo, hi}, bf16x2);
  return __builtin_bit_cast(unsigned, v);
}
__device__ __forceinline__ float relu(float v) { return __builtin_elementwise_maximum(v, 0.0f); }
#define MFMA(a, b, c) __builtin_amdgcn_mfma_f32_16x16x32_bf16((a), (b), (c), 0, 0, 0)
#define SGB(mask, n) __builtin_amdgcn_sched_group_barrier((mask), (n), 0)

__device__ __forceinline__ u64 ld_agent(const u64* p) { return __hip_atomic_load((GAS u64*)p, __ATOMIC_RELAXED, __HIP_MEMORY_SCOPE_AGENT); }
__device__ __forceinline__ void st_agent(u64* p, u64 v) { __hip_atomic_store((GAS u64*)p, v, __ATOMIC_RELAXED, __HIP_MEMORY_SCOPE_AGENT); }

template <int AUX> __device__ __forceinline__ void st128(u32x4 v, __amdgpu_buffer_rsrc_t r, unsigned voff, unsigned soff) {
  __builtin_amdgcn_raw_buffer_store_b128(v, r, voff, soff, AUX);
  asm volatile("s_nop 1" ::"v"(v) : "memory");
}
template <int MASK> __device__ __forceinline__ void st_flag(u64* p, u64 v) {
  asm volatile("s_mov_b64 exec, %2\n\tglobal_store_dwordx2 %0, %1, off sc1\n\ts_mov_b64 exec, -1" ::"v"(p), "v"(v), "n"(MASK) : "memory");
}

__device__ __forceinline__ bool round3(const u64* fp, u64 g1) {
        unsigned ok_;
#define CHK(a, b, c, d) "global_load_dwordx2 v[" c ":" d "], %1, off sc1\n\ts_waitcnt vmcnt(2)\n\tv_subrev_co_u32 v" a ", vcc, %2, v" a "\n\t" \
                        "v_subb_co_u32 v" b ", vcc, v" b ", %3, vcc\n\tv_cmp_gt_u64 vcc, 3, v[" a ":" b "]\n\ts_cmp_eq_u64 vcc, -1\n\ts_cbranch_scc1 LRND_%=\n\t"
        asm volatile(CHK("250", "251", "254", "255") CHK("252", "253", "250", "251") CHK("254", "255", "252", "253") "LRND_%=:\n\ts_cselect_b32 %0, 1, 0"
                     : "=s"(ok_) : "v"(fp), "s"((unsigned)g1), "v"((unsigned)(g1 >> 32)) : "memory", "vcc", "scc", "v250", "v251", "v252", "v253", "v254", "v255");
#undef CHK
        return ok_ != 0;
}

__global__ __launch_bounds__(512, 2) void mlp_fused(const char* __restrict__ x, const char* __restrict__ w1,
                                                    const char* __restrict__ w2, char* __restrict__ out, char* ws) {
  __shared__ __attribute__((aligned(1024))) char smem_[163840];
  LAS char* smem = (LAS char*)smem_;
  asm volatile("" ::"s"(smem) : "memory");
  unsigned cpf = 0;
  asm volatile("s_getpc_b64 s[40:41]\nLCODE_BEG:\n\ts_mov_b32 s42, LCODE_E1-LCODE_BEG\n\ts_max_i32 s42, s42, LCODE_E2-LCODE_BEG\n\t"
               "s_max_i32 s42, s42, 0\n\ts_and_b32 s41, s41, 0xffff\n\ts_mov_b32 s43, 0x20000\n\tv_lshlrev_b32 %0, 6, %1\n\t"
               "buffer_load_dword %0, %0, s[40:43], 0 offen"
               : "={v248}"(cpf) : "v"(threadIdx.x) : "memory", "scc", "s40", "s41", "s42", "s43");
  const int wave = __builtin_amdgcn_readfirstlane(threadIdx.x >> 6);
  const unsigned lane = threadIdx.x & 63;
  const int b = blockIdx.x, xcd = b & 7, rr = b >> 3;
  const int bi = (xcd & 1) * 8 + (rr & 7), bj = (xcd >> 1) * 4 + (rr >> 3);
  const int wg = bi * 16 + bj;

  STAMP_DECL
  STAMP(0);
  const int w = wave - 4;
  const i32x4 sx = make_srd(x, kXBytes), sw1 = make_srd(w1, kWBytes);
  const unsigned sx0 = bi * 128 * 2048, sw0 = bj * 448 - (bj & 1) * 64;
  const int rot = ROT ? 4 * ((bi >> 2) + (bj >> 2)) : 0;
  auto mk_x = [&](unsigned lane, unsigned (&vx)[4]) {
#pragma unroll
    for (int p = 0; p < 4; ++p)
      vx[p] = (32 * w + 8 * p + (lane >> 3)) * 2048 + (((lane & 7) ^ ((4 * p + (lane >> 4)) & 7)) << 4) - p * 1024;
  };
  auto mk_w = [&](unsigned lane, unsigned (&vw)[8]) {
    const unsigned hi = (lane >> 5) & 1, base = hi * 7168 + (lane & 1) * 16 + w * (16 * 7168), t0 = (((lane >> 1) & 15) ^ hi) << 5;
#pragma unroll
    for (int p = 0; p < 8; ++p) vw[p] = base + (t0 ^ ((((p & 1) * 2) | (p & 4)) << 5)) + (p * 14336 - (p & 3) * 1024);
  };
  auto go_x = [&](const unsigned (&vx)[4], int t) {
    const unsigned so = sx0 + ((t + rot) & 15) * 128;
    set_m0((t & 3) * 16384 + w * 4096);
    dma<0>(sx, vx[0], so); dma<1024>(sx, vx[1], so); dma<2048>(sx, vx[2], so); dma<3072>(sx, vx[3], so);
  };
  auto go_w = [&](const unsigned (&vw)[8], int t, unsigned base) {
    const unsigned so = sw0 + ((t + rot) & 15) * (64 * 7168);
    set_m0(base + w * 8192);
    dma<0>(sw1, vw[0], so); dma<1024>(sw1, vw[1], so); dma<2048>(sw1, vw[2], so); dma<3072>(sw1, vw[3], so);
    set_m0(base + w * 8192 + 4096);
    dma<0>(sw1, vw[4], so); dma<1024>(sw1, vw[5], so); dma<2048>(sw1, vw[6], so); dma<3072>(sw1, vw[7], so);
  };
  if (wave >= 4) {
    unsigned vx[4], vw[8], l2 = lane;
    PS(1);
    if (WF == 2) { mk_w(l2, vw); go_w(vw, 0, 98304); asm volatile("" : "+v"(l2)); }
    mk_x(WF == 2 ? l2 & 63 : lane, vx);
    if (WF != 1 || !(w & 1)) go_x(vx, 0);
    PS(2);
    if (WF != 2) { asm volatile("" : "+v"(l2)); mk_w(l2, vw); go_w(vw, 0, 98304); }
    if (WF == 1 && (w & 1)) go_x(vx, 0);
    PS(3);
    if (EARLY > 1) { go_x(vx, 1); go_w(vw, 1, 131072); }
  }
  int wave2 = wave;
  asm volatile("" : "+s"(wave2) :: "memory");
  auto load_tag = [&]() -> u64 {
    u64 g;
    asm volatile("s_load_dwordx2 %0, %1, 0x0\n\ts_waitcnt lgkmcnt(0)" : "=s"(g) : "s"(ws + kWsG + bi * 128) : "memory");
    return g;
  };

  auto touch_off = [&](int v) -> unsigned {
    constexpr unsigned kSkip = 0x7fffffffu, kM = TCH >= 3 ? 15 : 0;
    if (v < 2) { const unsigned k = v * 64 + lane; return k < 112 && !(k & kM) ? (bi * 112 + (bj & 8) * 7) * 8192 + k * 4096 : kSkip; }
    if (v == 2) return lane < 16 ? (lane & 7 & kM ? kSkip : (unsigned)kWsX + (wg ^ (lane & 8)) * 32768 + (lane & 7) * 4096)
                     : lane < 32 ? (unsigned)kWsHF + (bi * 16 + (bj & 8)) * 256 + (lane & 15) * 128
                     : lane < 40 ? (unsigned)kWsXF + (wg ^ ((lane & 4) * 2)) * 512 + (lane & 3) * 128 : kSkip;
    return lane & kM ? kSkip : (bi * 128 + lane * 2) * 2048 + (bj & 7) * 256;
  };
  if (LFIRST ? __builtin_expect(wave2 >= 4, 1) : wave2 >= 4) {
    if (!PRIOL) __builtin_amdgcn_s_setprio(PRIO);
    const i32x4 sw2 = make_srd(w2, kWBytes), sh = make_srd(ws + kWsH, kHBytes);
    unsigned vx[4], vw[8], l3 = lane;
    asm volatile("" : "+v"(l3));
    mk_x(l3, vx); mk_w(l3, vw);
    auto issue_x = [&](int t) { go_x(vx, t); };
    auto issue_w = [&](int t, unsigned base) { go_w(vw, t, base); };
    if (EARLY <= 1) { issue_x(1); issue_w(1, 131072); }
    const unsigned w2col = (bj & 7) * 256, vh = lane * 16;
    unsigned vo[4];
#pragma unroll
    for (int p = 0; p < 4; ++p) {
      const unsigned row = 4 * p + (lane >> 4), c = lane & 15;
      vo[p] = row * 2048 + (((c >> 1) ^ (row & 7)) << 5) + ((c & 1) << 4) - p * 1024;
    }
    PS(4);
    const u64 gen = load_tag();
    PS(5);
    asm volatile("s_waitcnt vmcnt(12)" : "+{v248}"(cpf)::"memory");
    PS(6);
    bar();
    STAMP(1);
    issue_x(2); issue_w(2, 65536);
    wait_vm<12>();
    bar();
    if (PRIOL == 2) __builtin_amdgcn_s_setprio(PRIO);
    issue_x(3); issue_w(3, 98304); issue_x(4);
    const __amdgpu_buffer_rsrc_t rx = __builtin_amdgcn_make_buffer_rsrc(ws + kWsX, 0, kXchBytes, 0x00020000);
    unsigned wb = 131072, pfd = 0;
#pragma unroll 1
    for (int t = 1; t < 12; ++t) {
      wait_vm<16>();
      bar();
      issue_w(t + 3, wb); issue_x(t + 4);
      if (t == PFT) {
        unsigned idx = w * 64 + lane; idx = idx < 224 ? idx : 223;
        asm volatile("buffer_load_dword %0, %1, %2, %3 offen" : "+v"(pfd) : "v"((idx >> 1) * 2048 + (idx & 1) * 128), "s"(sw2), "s"(((bj >> 3) * 1792 + bi * 112) * 2048 + w2col) : "memory");
      }
      wb = wb == 131072 ? 65536 : wb + 32768;
    }
    unsigned tabw, tab;
    {
      const unsigned pi = lane / 7, dd = lane % 7 + 1, tile = (bj & 8) | ((bj + dd) & 7);
      tabw = tile * 7 + (pi >> 1) + (pi & 1) * 4;
      tab = (w & 1) ? tabw : tile * 7 + pi;
    }
    constexpr int kP = PA + PB, kD = 4 + kP, kPC = PC;
    static_assert(PA >= 3 && kP % 2 == 0 && kD >= 16 && kD <= 26 && kPC % 2 == 0 && kD + kPC <= 30 && PC1 - PC0 <= 4 && kPC - PC1 <= 8 && B1 - B0 <= 4, "");
    const unsigned hi2 = w >> 1;
    auto phase2 = [&](auto odd_) {
    constexpr bool kOdd = decltype(odd_)::value;
    u32x4 pl[kP][2];
    const __amdgpu_buffer_rsrc_t rw2 = __builtin_amdgcn_make_buffer_rsrc((void*)w2, 0, kWBytes, 0x00020000);
    const unsigned vq0 = (kOdd ? vo[2] + 2048 : vo[0]) + (w >> 1) * 32768, vq1 = (kOdd ? vo[3] + 3072 : vo[1] + 1024) + (w >> 1) * 32768;
    auto preload = [&](int i0, int i1) {
#pragma unroll
      for (int i = i0; i < i1; ++i) {
        const unsigned kk = i < 3 ? bj * 7 + (i == 0 ? 2 : i == 1 ? 6 : 3) : __builtin_amdgcn_readlane(tabw, i - 3);
        pl[i][0] = __builtin_amdgcn_raw_buffer_load_b128(rw2, vq0, kk * 65536 + w2col, 0);
        pl[i][1] = __builtin_amdgcn_raw_buffer_load_b128(rw2, vq1, kk * 65536 + w2col, 0);
      }
    };
    auto deliver = [&](int n0, int n1) {
#pragma unroll
      for (int n = n0; n < n1; ++n)
        if (n < kD) {
          *(LAS u32x4*)(smem + (10 + (n + 6) % 10) * 8192 + w * 2048 + vh) = pl[n - 4][0];
          *(LAS u32x4*)(smem + (10 + (n + 6) % 10) * 8192 + w * 2048 + 1024 + vh) = pl[n - 4][1];
        }
    };
    if (PRIOL == 1) __builtin_amdgcn_s_setprio(PRIO);
    wait_vm<16>();
    bar();
    issue_w(15, wb);
    preload(0, PA);
    wait_vm<8 + W13 * 2 * PA>();
    bar();
    STAMP(2);
    auto issue_w2 = [&](unsigned unit, unsigned kk, unsigned half) {
      const unsigned so = kk * 65536 + w2col + half * 32768;
      set_m0(unit * 8192 + half * 4096);
      dma<0>(sw2, vo[0], so); dma<1024>(sw2, vo[1], so); dma<2048>(sw2, vo[2], so); dma<3072>(sw2, vo[3], so);
    };
    auto issue_h = [&](unsigned unit, unsigned idx, unsigned half) {
      const unsigned so = (bi * 112 + idx) * 8192 + half * 4096;
      set_m0(unit * 8192 + half * 4096);
      dma_sc1<0>(sh, vh, so); dma_sc1<1024>(sh, vh, so); dma_sc1<2048>(sh, vh, so); dma_sc1<3072>(sh, vh, so);
    };
    issue_w2(16 + hi2, bj * 7 + hi2 * 4, kOdd); issue_w2(18 + hi2, bj * 7 + 1 + hi2 * 4, kOdd);
    asm volatile("s_waitcnt vmcnt(%1)" : "+v"(pfd) : "n"(8) : "memory");
    bar();
    STAMP(3);
    preload(PA, PA + PB);
    u64* const myflag = (u64*)(ws + kWsHF + wg * 256 + hi2 * 128);
    const __amdgpu_buffer_rsrc_t rh = __builtin_amdgcn_make_buffer_rsrc(ws + kWsH, 0, kHBytes, 0x00020000);
    auto pub_rd = [&](unsigned off) { return *(LAS u32x4*)(smem + off + vh); };
    auto pub_st = [&](u32x4 v, unsigned off) { st128<16>(v, rh, vh + (off & 3072), (bi * 112 + bj * 7) * 8192 + (off & ~3072u)); };
    constexpr std::integral_constant<int, 8> k8{};
    unsigned pos = kOdd ? kD + kPC - 7 : 0, ring = kOdd ? (kD + kPC + 6) % 10 : 7;
    auto advance = [&]() { ring = ring == 9 ? 0 : ring + 1; ++pos; };
    auto next_w2 = [&]() { issue_w2(10 + ring, __builtin_amdgcn_readlane(tab, pos), hi2); advance(); };
    auto next_h = [&]() { issue_h(ring, __builtin_amdgcn_readlane(tab, pos), hi2); advance(); };
    wait_vm<2 * PB>();
    bar();
    STAMP(4);
    if constexpr (kOdd) {
      u32x4 v[8];
#pragma unroll
      for (int e = 0; e < 8; ++e) v[e] = pub_rd((e >> 2) * 8192 + hi2 * 4096 + (e & 3) * 1024);
#pragma unroll
      for (int e = 0; e < 8; ++e) pub_st(v[e], (e >> 2) * 8192 + hi2 * 4096 + (e & 3) * 1024);
    }
#pragma unroll
    for (int i = 0; i < 3; ++i) asm volatile("" : "+v"(pl[i][0]), "+v"(pl[i][1]));
    bar();
    auto landed = [&]() {
      wait_vm<0>();
#pragma unroll
      for (int i = 3; i < kP; ++i) asm volatile("" : "+v"(pl[i][0]), "+v"(pl[i][1]));
    };
    if (!kOdd || DLV1) deliver(4, 7);
    if (!kOdd || !FL3) landed();
    if constexpr (kOdd) {
      u32x4 vb[20];
      auto off_b = [&](int e) { return e < 16 ? 16384 + (e >> 3) * 16384 + hi2 * 8192 + (e & 7) * 1024 : 49152 + hi2 * 4096 + (e - 16) * 1024; };
      auto get_b = [&](int e0, int e1) {
#pragma unroll
        for (int e = e0; e < e1; ++e) vb[e] = pub_rd(off_b(e));
      };
      auto put_b = [&](int e0, int e1, auto slp) {
#pragma unroll
        for (int e = e0; e < e1; ++e) if (e < 20) {
          if (decltype(slp)::value) __builtin_amdgcn_s_sleep(decltype(slp)::value);
          pub_st(vb[e], off_b(e));
          asm volatile("" ::: "memory");
        }
      };
      u32x4 pc[kPC + 1][4];
      auto preload2 = [&](int i0, int i1) {
#pragma unroll
        for (int i = i0; i < i1; ++i) if (i < kPC) {
          const unsigned so = __builtin_amdgcn_readlane(tabw, kD - 7 + i) * 65536 + w2col + hi2 * 32768;
#pragma unroll
          for (int p = 0; p < 4; ++p) pc[i][p] = __builtin_amdgcn_raw_buffer_load_b128(rw2, vo[p] + p * 1024, so, 0);
          asm volatile("" ::: "memory");
        }
      };
      auto deliver2 = [&](int n) {
#pragma unroll
        for (int p = 0; p < 4; ++p) *(LAS u32x4*)(smem + (10 + (n + 6) % 10) * 8192 + hi2 * 4096 + p * 1024 + vh) = pc[n - kD][p];
      };
      constexpr std::integral_constant<int, BSL> ksl{};
      constexpr std::integral_constant<int, 0> k0{};
      if (!FL3) st_flag<1>(myflag, gen + 1);
      STAMP(5);
      if (!DLV1) deliver(4, 7);
      get_b(0, 8);
      preload2(0, PC0);
      put_b(0, B0, k0);
      bar();
      if (FL3 == 2) get_b(8, 16);
      if (FL3) { landed(); st_flag<1>(myflag, gen + 1); }
      STAMP(6);
      deliver(7, 10);
      if (FL3 != 2) get_b(8, 16);
#pragma unroll
      for (int e = 0; e < 4; ++e) { if (PC0 + e < PC1) preload2(PC0 + e, PC0 + e + 1); if (B0 + e < B1) put_b(B0 + e, B0 + e + 1, ksl); }
      wait_lgkm0();
      SA(7);
      bar();
      SA(8);
      get_b(16, 20);
      deliver(10, 14);
#pragma unroll
      for (int e = 0; e < 8; ++e) { preload2(PC1 + e, PC1 + e + 1); if (B1 + e < 8) put_b(B1 + e, B1 + e + 1, ksl); }
      wait_lgkm0();
      SA(9);
      bar();
      SA(10); SB(7);
#pragma unroll
      for (int k = 5; k <= 19; k += 2) {
        if (k == 7 || k == 17) {
          wait_vm<0>();
#pragma unroll
          for (int i = 0; i < kPC; ++i) if (k == 7) asm volatile("" : "+v"(pc[i][0]), "+v"(pc[i][1]), "+v"(pc[i][2]), "+v"(pc[i][3]));
          st_flag<1>(myflag, gen + (k == 7 ? 2 : 3));
        }
        if (k + 9 < kD) deliver(k + 9, k + 11);
        else if (k + 9 < kD + kPC) { deliver2(k + 9); deliver2(k + 10); }
        else { next_w2(); next_w2(); }
        if (k >= 7) put_b(8 + (k - 7) / 2 * 3, 8 + (k - 5) / 2 * 3, k0);
        wait_vm<24>();
        wait_lgkm0();
        barG();
        if (k == 7) SB(8);
        if (k == 15) SB(9);
      }
#pragma unroll 1
      for (int k = 21; k <= 45; k += 2) {
        next_w2(); next_w2();
        wait_vm<24>();
        barG();
        if (k == 27) SB(10);
      }
    } else {
      const unsigned fl = OWNX && ((lane >> 1) & 7) == (bj & 7) ? (lane & 15) ^ 2 : lane & 15;
      const u64* fp = (const u64*)(ws + kWsHF + (bi * 16 + (bj & 8)) * 256 + fl * 128);
      auto reached = [&](u64 f, unsigned lv) { return __all(f - gen - lv <= (u64)(3 - lv)); };
      auto poll = [&](unsigned lv) {
        _Pragma("unroll 1") for (int spin = 0; spin < kSpinCap; ++spin) {
          if (reached(ld_agent(fp), lv)) break;
          __builtin_amdgcn_s_sleep(1);
        }
        asm volatile("" ::: "memory");
      };
      auto peek = [&]() { asm volatile("global_load_dwordx2 v[250:251], %0, off sc1" ::"v"(fp) : "memory", "v250", "v251"); };
      auto peeked = [&](auto nv) {
        unsigned lo, hi;
        asm volatile("s_waitcnt vmcnt(%2)\n\tv_mov_b32 %0, v250\n\tv_mov_b32 %1, v251" : "=v"(lo), "=v"(hi) : "n"(decltype(nv)::value) : "memory", "v250", "v251");
        return ((u64)hi << 32) | lo;
      };
      const u64 g1 = gen + 1;
      auto round = [&]() -> bool { return round3(fp, g1); };
      __builtin_amdgcn_s_sleep(PKS);
      peek();
      bar();
      STAMP(6);
      asm volatile("global_load_dwordx2 v[252:253], %0, off sc1" ::"v"(fp) : "memory", "v252", "v253");
      deliver(7, 10);
      bool ok = round();
      if (ok) { next_h(); next_h(); next_h(); }
      wait_lgkm0();
      STAMP(7);
      bar();
      deliver(10, 14);
      if (!ok) {
        _Pragma("unroll 1") for (int spin = 0; spin < kSpinCap; ++spin) if (round()) break;
        STAMP(8);
        next_h(); next_h(); next_h();
        wait_vm<8>();
      } else {
        STAMP(8);
        next_h(); next_h(); next_h(); next_h();
        wait_vm<24>();
      }
      STAMP(9);
      bar();
      STAMP(10);
      auto step = [&](int k) {
        if (k == 27 && !reached(peeked(k8), 3)) poll(3);
        if (k == 25) peek();
        next_h(); next_h();
        wait_vm<16>();
      };
#pragma unroll
      for (int k = 5; k <= 15; k += 2) {
        deliver(k + 9, k + 11);
        if (k <= 9) {
          next_h(); next_h();
          if (ok) wait_vm<24>();
          else if (k == 5) wait_vm<8>();
          else { next_h(); next_h(); if (k == 7) wait_vm<16>(); else wait_vm<24>(); }
        }
        else if (k == 11) { __builtin_amdgcn_s_sleep(2); peek(); wait_vm<17>(); }
        else {
          if (k == 13 && !reached(peeked(std::integral_constant<int, 0>{}), 2)) poll(2);
          step(k);
        }
        wait_lgkm0();
        barG();
      }
#pragma unroll 1
      for (int k = 17; k <= 45; k += 2) {
        step(k);
        barG();
      }
      next_h(); next_h();
    }
    };
    if (w & 1) phase2(std::true_type{}); else phase2(std::false_type{});
    wait_vm<16>();
    barG();
    wait_vm<8>();
    barG();
    wait_vm<0>();
    barG();
    {
      u32x4 rcv[8];
      unsigned rd0 = (lane >> 3) * 128 + (((lane & 7) ^ (lane >> 4)) << 4), rd1 = (rd0 ^ 64) + 1024, ovo = (lane >> 3) * 2048 + (lane & 7) * 16;
      asm volatile("" : "+v"(rd0), "+v"(rd1), "+v"(ovo));
      f32x4 keep[8];
      auto fetch = [&]() {
        asm volatile("" ::: "memory");
#pragma unroll
        for (int e = 0; e < (XBF ? 4 : 8); ++e) rcv[e] = __builtin_amdgcn_raw_buffer_load_b128(rx, vh + (e & 3) * 1024, wg * 32768 + w * 8192 + (e >> 2) * 4096, 16);
      };
      auto kept = [&]() {
        bar();
#pragma unroll
        for (int e = 0; e < 8; ++e) keep[e] = *(LAS f32x4*)(smem + 98304 + w * 8192 + e * 1024 + vh);
      };
      if (!TL2) kept();
      const u64* xp = (const u64*)(ws + kWsXF + wg * 512 + w * XST);
      if (XRND) {
        asm volatile("" : "+v"(xp));
        asm volatile("global_load_dwordx2 v[250:251], %0, off sc1\n\ts_sleep 2\n\tglobal_load_dwordx2 v[252:253], %0, off sc1\n\ts_sleep 2" ::"v"(xp) : "memory", "v250", "v251", "v252", "v253");
        _Pragma("unroll 1") for (int spin = 0; spin < kSpinCap; ++spin) if (round3(xp, gen + 1)) break;
      } else {
        _Pragma("unroll 1") for (int spin = 0; spin < kSpinCap; ++spin) {
          if (__all(ld_agent(xp) == gen + 1)) break;
          __builtin_amdgcn_s_sleep(1);
        }
      }
      fetch();
      if (TL2) kept();
      STAMP(11);
      LAS char* tp = smem + 131072 + w * 4096;
      const unsigned r16 = lane & 15, q = lane >> 4;
#pragma unroll
      for (int e = 0; e < 8; ++e) {
        const unsigned r0 = rcv[e >> 1][(e & 1) * 2], r1 = rcv[e >> 1][(e & 1) * 2 + 1];
        const f32x4 o = keep[e] + __builtin_bit_cast(f32x4, XBF ? (u32x4){r0 << 16, r0 & 0xffff0000u, r1 << 16, r1 & 0xffff0000u} : rcv[e]);
        *(LAS u32x2*)(tp + (e >> 2) * 2048 + r16 * 128 + ((((e & 3) * 2 + (q >> 1)) ^ (r16 >> 1)) << 4) + (q & 1) * 8) = (u32x2){pack_bf16(o[0], o[1]), pack_bf16(o[2], o[3])};
      }
      const __amdgpu_buffer_rsrc_t ro = __builtin_amdgcn_make_buffer_rsrc(out, 0, kOBytes, 0x00020000);
      const unsigned obase = (bi * 128 + hi2 * 64 + (bj >> 3) * 32) * 2048 + ((bj & 7) * 128 + (w & 1) * 64) * 2;
      u32x4 ov[4];
#pragma unroll
      for (int e = 0; e < 4; ++e) ov[e] = *(LAS u32x4*)(tp + (e >> 1) * 2048 + ((e & 1) ? rd1 : rd0));
#pragma unroll
      for (int e = 0; e < 4; ++e) st128<OAUX>(ov[e], ro, ovo, obase + e * 16384);
      if (bj == 0 && w == 0) st_flag<1>((u64*)(ws + kWsG + bi * 128), gen + 4);
    }
    STAMP_FLUSH();
    asm volatile("LCODE_E1:");
    return;
  }

  const int wm = wave >> 1, wn = wave & 1;
  const unsigned r16 = lane & 15, q = lane >> 4, q4 = (lane >> 2) & 3, p4 = lane & 3;
  unsigned tch = 0;
  {
    const i32x4 st = wave == 3 ? make_srd(out, kOBytes) : make_srd(ws, 32u << 20);
    asm volatile("s_sleep %3\n\tbuffer_load_dword %0, %1, %2, 0 offen sc1" : "={v249}"(tch) : "v"(touch_off(wave)), "s"(st), "n"(TSL) : "memory");
  }
  const u64 gen = load_tag();
  f32x32 accA, accB;
  {
    unsigned ax0 = (wm * 64 + r16) * 128 + ((q ^ ((r16 >> 1) & 7)) << 4), ax1 = ax0 ^ 64;
    unsigned aw[7], a2[4];
#pragma unroll
    for (int j = 0; j < 7; ++j) {
      const unsigned fb = wn ? (j < 6 ? 8 + j : 7) : j;
      const unsigned u = (bj & 1) * 2 + fb, s = q4 | ((q & 1) << 2);
      aw[j] = 98304 + (8 * q + q4) * 512 + ((u ^ s) << 5) + p4 * 8;
    }
    unsigned ah = wm * 4096 + lane * 16;
    const unsigned hw = ah + wn * 8192, hw6 = ah + 6 * 8192 + wn * 8;
#pragma unroll
    for (int n = 0; n < 4; ++n) a2[n] = 16 * 8192 + (4 * q + q4) * 256 + (((wn * 4 + n) ^ ((4 * q + q4) & 7)) << 5) + p4 * 8;
    unsigned cnt, dl, ps, xs;
    asm volatile(
"v_mov_b32 v0, 0\nv_mov_b32 v1, 0\nv_mov_b32 v2, 0\nv_mov_b32 v3, 0\nv_mov_b32 v4, 0\nv_mov_b32 v5, 0\nv_mov_b32 v6, 0\nv_mov_b32 v7, 0\nv_mov_b32 v8, 0\n"
"v_mov_b32 v9, 0\nv_mov_b32 v10, 0\nv_mov_b32 v11, 0\nv_mov_b32 v12, 0\nv_mov_b32 v13, 0\nv_mov_b32 v14, 0\nv_mov_b32 v15, 0\nv_mov_b32 v16, 0\nv_mov_b32 v17, 0\n"
"v_mov_b32 v18, 0\nv_mov_b32 v19, 0\nv_mov_b32 v20, 0\nv_mov_b32 v21, 0\nv_mov_b32 v22, 0\nv_mov_b32 v23, 0\nv_mov_b32 v24, 0\nv_mov_b32 v25, 0\nv_mov_b32 v26, 0\n"
"v_mov_b32 v27, 0\nv_mov_b32 v28, 0\nv_mov_b32 v29, 0\nv_mov_b32 v30, 0\nv_mov_b32 v31, 0\nv_mov_b32 v32, 0\nv_mov_b32 v33, 0\nv_mov_b32 v34, 0\nv_mov_b32 v35, 0\n"
"v_mov_b32 v36, 0\nv_mov_b32 v37, 0\nv_mov_b32 v38, 0\nv_mov_b32 v39, 0\nv_mov_b32 v40, 0\nv_mov_b32 v41, 0\nv_mov_b32 v42, 0\nv_mov_b32 v43, 0\nv_mov_b32 v44, 0\n"
"v_mov_b32 v45, 0\nv_mov_b32 v46, 0\nv_mov_b32 v47, 0\nv_mov_b32 v48, 0\nv_mov_b32 v49, 0\nv_mov_b32 v50, 0\nv_mov_b32 v51, 0\nv_mov_b32 v52, 0\nv_mov_b32 v53, 0\n"
"v_mov_b32 v54, 0\nv_mov_b32 v55, 0\nv_mov_b32 v56, 0\nv_mov_b32 v57, 0\nv_mov_b32 v58, 0\nv_mov_b32 v59, 0\nv_mov_b32 v60, 0\nv_mov_b32 v61, 0\nv_mov_b32 v62, 0\n"
"v_mov_b32 v63, 0\nv_mov_b32 v64, 0\nv_mov_b32 v65, 0\nv_mov_b32 v66, 0\nv_mov_b32 v67, 0\nv_mov_b32 v68, 0\nv_mov_b32 v69, 0\nv_mov_b32 v70, 0\nv_mov_b32 v71, 0\n"
"v_mov_b32 v72, 0\nv_mov_b32 v73, 0\nv_mov_b32 v74, 0\nv_mov_b32 v75, 0\nv_mov_b32 v76, 0\nv_mov_b32 v77, 0\nv_mov_b32 v78, 0\nv_mov_b32 v79, 0\nv_mov_b32 v80, 0\n"
"v_mov_b32 v81, 0\nv_mov_b32 v82, 0\nv_mov_b32 v83, 0\nv_mov_b32 v84, 0\nv_mov_b32 v85, 0\nv_mov_b32 v86, 0\nv_mov_b32 v87, 0\nv_mov_b32 v88, 0\nv_mov_b32 v89, 0\n"
"v_mov_b32 v90, 0\nv_mov_b32 v91, 0\nv_mov_b32 v92, 0\nv_mov_b32 v93, 0\nv_mov_b32 v94, 0\nv_mov_b32 v95, 0\nv_mov_b32 v96, 0\nv_mov_b32 v97, 0\nv_mov_b32 v98, 0\n"
"v_mov_b32 v99, 0\nv_mov_b32 v100, 0\nv_mov_b32 v101, 0\nv_mov_b32 v102, 0\nv_mov_b32 v103, 0\nv_mov_b32 v104, 0\nv_mov_b32 v105, 0\nv_mov_b32 v106, 0\n"
"v_mov_b32 v107, 0\nv_mov_b32 v108, 0\nv_mov_b32 v109, 0\nv_mov_b32 v110, 0\nv_mov_b32 v111, 0\ns_mov_b32 %[cnt], 0\ns_mov_b32 %[ps], 0x8000\ns_mov_b32 %[dl], 0x8000\n"
"s_mov_b32 %[xs], 0x4000\ns_cmp_eq_u32 %[cnt], 13\ns_barrier\nds_read_b128 v[112:115], v224\nds_read_b64_tr_b16 v[128:129], v226\nds_read_b64_tr_b16 v[130:131], v226 offset:2048\n"
"ds_read_b128 v[116:119], v224 offset:2048\nds_read_b128 v[120:123], v224 offset:4096\nds_read_b128 v[124:127], v224 offset:6144\nds_read_b64_tr_b16 v[132:133], v227\n"
"ds_read_b64_tr_b16 v[134:135], v227 offset:2048\nds_read_b64_tr_b16 v[136:137], v228\nds_read_b64_tr_b16 v[138:139], v228 offset:2048\nds_read_b64_tr_b16 v[140:141], v229\n"
"ds_read_b64_tr_b16 v[142:143], v229 offset:2048\nds_read_b64_tr_b16 v[144:145], v230\nds_read_b64_tr_b16 v[146:147], v230 offset:2048\nds_read_b64_tr_b16 v[148:149], v231\n"
"ds_read_b64_tr_b16 v[150:151], v231 offset:2048\nds_read_b64_tr_b16 v[152:153], v232\nds_read_b64_tr_b16 v[154:155], v232 offset:2048\nL1_%=:\ns_waitcnt lgkmcnt(0)\n"
"v_mfma_f32_16x16x32_bf16 v[0:3], v[128:131], v[112:115], v[0:3]\nds_read_b128 v[156:159], v225\n"
"v_mfma_f32_16x16x32_bf16 v[4:7], v[128:131], v[116:119], v[4:7]\nds_read_b64_tr_b16 v[172:173], v226 offset:16384\n"
"v_mfma_f32_16x16x32_bf16 v[8:11], v[128:131], v[120:123], v[8:11]\nds_read_b64_tr_b16 v[174:175], v226 offset:18432\n"
"v_mfma_f32_16x16x32_bf16 v[12:15], v[128:131], v[124:127], v[12:15]\nv_add_u32 v226, %[dl], v226\n"
"v_mfma_f32_16x16x32_bf16 v[16:19], v[132:135], v[112:115], v[16:19]\nds_read_b128 v[160:163], v225 offset:2048\n"
"v_mfma_f32_16x16x32_bf16 v[20:23], v[132:135], v[116:119], v[20:23]\nds_read_b64_tr_b16 v[176:177], v227 offset:16384\n"
"v_mfma_f32_16x16x32_bf16 v[24:27], v[132:135], v[120:123], v[24:27]\nds_read_b64_tr_b16 v[178:179], v227 offset:18432\n"
"v_mfma_f32_16x16x32_bf16 v[28:31], v[132:135], v[124:127], v[28:31]\nv_add_u32 v227, %[dl], v227\n"
"v_mfma_f32_16x16x32_bf16 v[32:35], v[136:139], v[112:115], v[32:35]\nds_read_b128 v[164:167], v225 offset:4096\n"
"v_mfma_f32_16x16x32_bf16 v[36:39], v[136:139], v[116:119], v[36:39]\nds_read_b64_tr_b16 v[180:181], v228 offset:16384\n"
"v_mfma_f32_16x16x32_bf16 v[40:43], v[136:139], v[120:123], v[40:43]\nds_read_b64_tr_b16 v[182:183], v228 offset:18432\n"
"v_mfma_f32_16x16x32_bf16 v[44:47], v[136:139], v[124:127], v[44:47]\nv_add_u32 v228, %[dl], v228\n"
"v_mfma_f32_16x16x32_bf16 v[48:51], v[140:143], v[112:115], v[48:51]\nds_read_b128 v[168:171], v225 offset:6144\n"
"v_mfma_f32_16x16x32_bf16 v[52:55], v[140:143], v[116:119], v[52:55]\nds_read_b64_tr_b16 v[184:185], v229 offset:16384\n"
"v_mfma_f32_16x16x32_bf16 v[56:59], v[140:143], v[120:123], v[56:59]\nds_read_b64_tr_b16 v[186:187], v229 offset:18432\n"
"v_mfma_f32_16x16x32_bf16 v[60:63], v[140:143], v[124:127], v[60:63]\nv_add_u32 v229, %[dl], v229\n"
"v_mfma_f32_16x16x32_bf16 v[64:67], v[144:147], v[112:115], v[64:67]\nds_read_b64_tr_b16 v[188:189], v230 offset:16384\n"
"v_mfma_f32_16x16x32_bf16 v[68:71], v[144:147], v[116:119], v[68:71]\nds_read_b64_tr_b16 v[190:191], v230 offset:18432\n"
"v_mfma_f32_16x16x32_bf16 v[72:75], v[144:147], v[120:123], v[72:75]\nds_read_b64_tr_b16 v[192:193], v231 offset:16384\n"
"v_mfma_f32_16x16x32_bf16 v[76:79], v[144:147], v[124:127], v[76:79]\nds_read_b64_tr_b16 v[194:195], v231 offset:18432\n"
"v_mfma_f32_16x16x32_bf16 v[80:83], v[148:151], v[112:115], v[80:83]\nds_read_b64_tr_b16 v[196:197], v232 offset:16384\n"
"v_mfma_f32_16x16x32_bf16 v[84:87], v[148:151], v[116:119], v[84:87]\nds_read_b64_tr_b16 v[198:199], v232 offset:18432\n"
"v_mfma_f32_16x16x32_bf16 v[88:91], v[148:151], v[120:123], v[88:91]\nv_add_u32 v230, %[dl], v230\n"
"v_mfma_f32_16x16x32_bf16 v[92:95], v[148:151], v[124:127], v[92:95]\nv_add_u32 v231, %[dl], v231\n"
"v_mfma_f32_16x16x32_bf16 v[96:99], v[152:155], v[112:115], v[96:99]\nv_add_u32 v232, %[dl], v232\n"
"v_mfma_f32_16x16x32_bf16 v[100:103], v[152:155], v[116:119], v[100:103]\nv_add_u16 v224, %[xs], v224\n"
"v_mfma_f32_16x16x32_bf16 v[104:107], v[152:155], v[120:123], v[104:107]\nv_add_u16 v225, %[xs], v225\n"
"v_mfma_f32_16x16x32_bf16 v[108:111], v[152:155], v[124:127], v[108:111]\ns_waitcnt lgkmcnt(0)\ns_barrier\ns_cbranch_scc1 L2_%=\n"
"v_mfma_f32_16x16x32_bf16 v[0:3], v[172:175], v[156:159], v[0:3]\nds_read_b128 v[112:115], v224\n"
"v_mfma_f32_16x16x32_bf16 v[4:7], v[172:175], v[160:163], v[4:7]\nds_read_b64_tr_b16 v[128:129], v226\n"
"v_mfma_f32_16x16x32_bf16 v[8:11], v[172:175], v[164:167], v[8:11]\nds_read_b64_tr_b16 v[130:131], v226 offset:2048\n"
"v_mfma_f32_16x16x32_bf16 v[12:15], v[172:175], v[168:171], v[12:15]\n"
"v_mfma_f32_16x16x32_bf16 v[16:19], v[176:179], v[156:159], v[16:19]\nds_read_b128 v[116:119], v224 offset:2048\n"
"v_mfma_f32_16x16x32_bf16 v[20:23], v[176:179], v[160:163], v[20:23]\nds_read_b64_tr_b16 v[132:133], v227\n"
"v_mfma_f32_16x16x32_bf16 v[24:27], v[176:179], v[164:167], v[24:27]\nds_read_b64_tr_b16 v[134:135], v227 offset:2048\n"
"v_mfma_f32_16x16x32_bf16 v[28:31], v[176:179], v[168:171], v[28:31]\n"
"v_mfma_f32_16x16x32_bf16 v[32:35], v[180:183], v[156:159], v[32:35]\nds_read_b128 v[120:123], v224 offset:4096\n"
"v_mfma_f32_16x16x32_bf16 v[36:39], v[180:183], v[160:163], v[36:39]\nds_read_b64_tr_b16 v[136:137], v228\n"
"v_mfma_f32_16x16x32_bf16 v[40:43], v[180:183], v[164:167], v[40:43]\nds_read_b64_tr_b16 v[138:139], v228 offset:2048\n"
"v_mfma_f32_16x16x32_bf16 v[44:47], v[180:183], v[168:171], v[44:47]\n"
"v_mfma_f32_16x16x32_bf16 v[48:51], v[184:187], v[156:159], v[48:51]\nds_read_b128 v[124:127], v224 offset:6144\n"
"v_mfma_f32_16x16x32_bf16 v[52:55], v[184:187], v[160:163], v[52:55]\nds_read_b64_tr_b16 v[140:141], v229\n"
"v_mfma_f32_16x16x32_bf16 v[56:59], v[184:187], v[164:167], v[56:59]\nds_read_b64_tr_b16 v[142:143], v229 offset:2048\n"
"v_mfma_f32_16x16x32_bf16 v[60:63], v[184:187], v[168:171], v[60:63]\n"
"v_mfma_f32_16x16x32_bf16 v[64:67], v[188:191], v[156:159], v[64:67]\nds_read_b64_tr_b16 v[144:145], v230\n"
"v_mfma_f32_16x16x32_bf16 v[68:71], v[188:191], v[160:163], v[68:71]\nds_read_b64_tr_b16 v[146:147], v230 offset:2048\n"
"v_mfma_f32_16x16x32_bf16 v[72:75], v[188:191], v[164:167], v[72:75]\nds_read_b64_tr_b16 v[148:149], v231\n"
"v_mfma_f32_16x16x32_bf16 v[76:79], v[188:191], v[168:171], v[76:79]\nds_read_b64_tr_b16 v[150:151], v231 offset:2048\n"
"v_mfma_f32_16x16x32_bf16 v[80:83], v[192:195], v[156:159], v[80:83]\nds_read_b64_tr_b16 v[152:153], v232\n"
"v_mfma_f32_16x16x32_bf16 v[84:87], v[192:195], v[160:163], v[84:87]\nds_read_b64_tr_b16 v[154:155], v232 offset:2048\n"
"v_mfma_f32_16x16x32_bf16 v[88:91], v[192:195], v[164:167], v[88:91]\ns_add_u32 %[cnt], %[cnt], 1\n"
"v_mfma_f32_16x16x32_bf16 v[92:95], v[192:195], v[168:171], v[92:95]\ns_bitcmp1_b32 0x2492, %[cnt]\n"
"v_mfma_f32_16x16x32_bf16 v[96:99], v[196:199], v[156:159], v[96:99]\ns_cselect_b32 %[dl], 0xffff0000, %[ps]\n"
"v_mfma_f32_16x16x32_bf16 v[100:103], v[196:199], v[160:163], v[100:103]\ns_cmp_eq_u32 %[cnt], 13\n"
"v_mfma_f32_16x16x32_bf16 v[104:107], v[196:199], v[164:167], v[104:107]\n"
"v_mfma_f32_16x16x32_bf16 v[108:111], v[196:199], v[168:171], v[108:111]\ns_branch L1_%=\nL2_%=:\n"
"v_mfma_f32_16x16x32_bf16 v[0:3], v[172:175], v[156:159], v[0:3]\nds_read_b128 v[112:115], v224\n"
"v_mfma_f32_16x16x32_bf16 v[4:7], v[172:175], v[160:163], v[4:7]\nds_read_b128 v[116:119], v225\n"
"v_mfma_f32_16x16x32_bf16 v[8:11], v[172:175], v[164:167], v[8:11]\n"
"v_mfma_f32_16x16x32_bf16 v[12:15], v[172:175], v[168:171], v[12:15]\nds_read_b128 v[120:123], v224 offset:16384\n"
"v_mfma_f32_16x16x32_bf16 v[16:19], v[176:179], v[156:159], v[16:19]\nds_read_b128 v[124:127], v225 offset:16384\n"
"v_mfma_f32_16x16x32_bf16 v[20:23], v[176:179], v[160:163], v[20:23]\nds_read_b64_tr_b16 v[172:173], v226\n"
"v_mfma_f32_16x16x32_bf16 v[24:27], v[176:179], v[164:167], v[24:27]\nds_read_b128 v[128:131], v224 offset:2048\n"
"v_mfma_f32_16x16x32_bf16 v[28:31], v[176:179], v[168:171], v[28:31]\nds_read_b64_tr_b16 v[174:175], v226 offset:2048\n"
"v_mfma_f32_16x16x32_bf16 v[32:35], v[180:183], v[156:159], v[32:35]\nds_read_b128 v[132:135], v225 offset:2048\n"
"v_mfma_f32_16x16x32_bf16 v[36:39], v[180:183], v[160:163], v[36:39]\nds_read_b64_tr_b16 v[176:177], v226 offset:16384\n"
"v_mfma_f32_16x16x32_bf16 v[40:43], v[180:183], v[164:167], v[40:43]\nds_read_b128 v[136:139], v224 offset:18432\n"
"v_mfma_f32_16x16x32_bf16 v[44:47], v[180:183], v[168:171], v[44:47]\nds_read_b64_tr_b16 v[178:179], v226 offset:18432\n"
"v_mfma_f32_16x16x32_bf16 v[48:51], v[184:187], v[156:159], v[48:51]\nds_read_b128 v[140:143], v225 offset:18432\n"
"v_mfma_f32_16x16x32_bf16 v[52:55], v[184:187], v[160:163], v[52:55]\n"
"v_mfma_f32_16x16x32_bf16 v[56:59], v[184:187], v[164:167], v[56:59]\nds_read_b128 v[144:147], v224 offset:4096\n"
"v_mfma_f32_16x16x32_bf16 v[60:63], v[184:187], v[168:171], v[60:63]\nds_read_b128 v[148:151], v225 offset:4096\n"
"v_mfma_f32_16x16x32_bf16 v[64:67], v[188:191], v[156:159], v[64:67]\n"
"v_mfma_f32_16x16x32_bf16 v[68:71], v[188:191], v[160:163], v[68:71]\nds_read_b128 v[152:155], v224 offset:20480\n"
"v_mfma_f32_16x16x32_bf16 v[72:75], v[188:191], v[164:167], v[72:75]\nds_read_b128 v[200:203], v225 offset:20480\n"
"v_mfma_f32_16x16x32_bf16 v[76:79], v[188:191], v[168:171], v[76:79]\ns_waitcnt vmcnt(0)\n"
"v_mfma_f32_16x16x32_bf16 v[80:83], v[192:195], v[156:159], v[80:83]\ns_barrier\n"
"v_mfma_f32_16x16x32_bf16 v[84:87], v[192:195], v[160:163], v[84:87]\nds_read_b64_tr_b16 v[180:181], v226 offset:32768\n"
"v_mfma_f32_16x16x32_bf16 v[88:91], v[192:195], v[164:167], v[88:91]\nds_read_b64_tr_b16 v[182:183], v226 offset:34816\n"
"v_mfma_f32_16x16x32_bf16 v[92:95], v[192:195], v[168:171], v[92:95]\nds_read_b64_tr_b16 v[184:185], v226 offset:49152\n"
"v_mfma_f32_16x16x32_bf16 v[96:99], v[196:199], v[156:159], v[96:99]\nds_read_b64_tr_b16 v[186:187], v226 offset:51200\n"
"v_mfma_f32_16x16x32_bf16 v[100:103], v[196:199], v[160:163], v[100:103]\nds_read_b128 v[204:207], v224 offset:6144\n"
"v_mfma_f32_16x16x32_bf16 v[104:107], v[196:199], v[164:167], v[104:107]\nds_read_b128 v[208:211], v225 offset:6144\n"
"v_mfma_f32_16x16x32_bf16 v[108:111], v[196:199], v[168:171], v[108:111]\nds_read_b128 v[212:215], v224 offset:22528\ns_waitcnt lgkmcnt(7)\n"
"v_mfma_f32_16x16x32_bf16 v[0:3], v[172:175], v[112:115], v[0:3]\nds_read_b64_tr_b16 v[156:157], v227\nds_read_b64_tr_b16 v[158:159], v227 offset:2048\n"
"v_mfma_f32_16x16x32_bf16 v[4:7], v[172:175], v[128:131], v[4:7]\nds_read_b128 v[216:219], v225 offset:22528\n"
"v_mfma_f32_16x16x32_bf16 v[0:3], v[176:179], v[116:119], v[0:3]\nds_read_b64_tr_b16 v[188:189], v228\nds_read_b64_tr_b16 v[190:191], v228 offset:2048\n"
"v_mfma_f32_16x16x32_bf16 v[4:7], v[176:179], v[132:135], v[4:7]\ns_waitcnt lgkmcnt(8)\nds_read_b64_tr_b16 v[192:193], v228 offset:16384\nds_read_b64_tr_b16 v[194:195], v228 offset:18432\n"
"v_mfma_f32_16x16x32_bf16 v[0:3], v[180:183], v[120:123], v[0:3]\nds_read_b64_tr_b16 v[196:197], v228 offset:32768\nds_read_b64_tr_b16 v[198:199], v228 offset:34816\n"
"v_mfma_f32_16x16x32_bf16 v[4:7], v[180:183], v[136:139], v[4:7]\nds_read_b64_tr_b16 v[220:221], v228 offset:49152\nds_read_b64_tr_b16 v[222:223], v228 offset:51200\n"
"v_mfma_f32_16x16x32_bf16 v[0:3], v[184:187], v[124:127], v[0:3]\ns_nop 1\n"
"v_mfma_f32_16x16x32_bf16 v[4:7], v[184:187], v[140:143], v[4:7]\ns_waitcnt lgkmcnt(8)\nds_read_b64_tr_b16 v[160:161], v227 offset:16384\nds_read_b64_tr_b16 v[162:163], v227 offset:18432\n"
"v_mfma_f32_16x16x32_bf16 v[8:11], v[172:175], v[144:147], v[8:11]\nds_read_b64_tr_b16 v[164:165], v227 offset:32768\nds_read_b64_tr_b16 v[166:167], v227 offset:34816\n"
"v_mfma_f32_16x16x32_bf16 v[12:15], v[172:175], v[204:207], v[12:15]\nv_maximum3_f32 v0, v0, 0, 0\nv_maximum3_f32 v1, v1, 0, 0\n"
"v_mfma_f32_16x16x32_bf16 v[8:11], v[176:179], v[148:151], v[8:11]\nv_maximum3_f32 v2, v2, 0, 0\nv_maximum3_f32 v3, v3, 0, 0\n"
"v_mfma_f32_16x16x32_bf16 v[12:15], v[176:179], v[208:211], v[12:15]\nv_cvt_pk_bf16_f32 v0, v0, v1\nv_cvt_pk_bf16_f32 v1, v2, v3\n"
"v_mfma_f32_16x16x32_bf16 v[8:11], v[180:183], v[152:155], v[8:11]\nv_maximum3_f32 v4, v4, 0, 0\nv_maximum3_f32 v5, v5, 0, 0\n"
"v_mfma_f32_16x16x32_bf16 v[12:15], v[180:183], v[212:215], v[12:15]\nv_maximum3_f32 v6, v6, 0, 0\nv_maximum3_f32 v7, v7, 0, 0\n"
"v_mfma_f32_16x16x32_bf16 v[8:11], v[184:187], v[200:203], v[8:11]\nv_cvt_pk_bf16_f32 v4, v4, v5\nv_cvt_pk_bf16_f32 v5, v6, v7\n"
"v_mfma_f32_16x16x32_bf16 v[12:15], v[184:187], v[216:219], v[12:15]\ns_waitcnt lgkmcnt(0)\nds_read_b64_tr_b16 v[168:169], v227 offset:49152\nds_read_b64_tr_b16 v[170:171], v227 offset:51200\n"
"v_mfma_f32_16x16x32_bf16 v[16:19], v[156:159], v[112:115], v[16:19]\nds_read_b64_tr_b16 v[172:173], v229\nds_read_b64_tr_b16 v[174:175], v229 offset:2048\n"
"v_mfma_f32_16x16x32_bf16 v[20:23], v[156:159], v[128:131], v[20:23]\nv_maximum3_f32 v8, v8, 0, 0\nv_maximum3_f32 v9, v9, 0, 0\n"
"v_mfma_f32_16x16x32_bf16 v[16:19], v[160:163], v[116:119], v[16:19]\nv_maximum3_f32 v10, v10, 0, 0\nv_maximum3_f32 v11, v11, 0, 0\n"
"v_mfma_f32_16x16x32_bf16 v[20:23], v[160:163], v[132:135], v[20:23]\nv_cvt_pk_bf16_f32 v8, v8, v9\nv_cvt_pk_bf16_f32 v9, v10, v11\n"
"v_mfma_f32_16x16x32_bf16 v[16:19], v[164:167], v[120:123], v[16:19]\nv_maximum3_f32 v12, v12, 0, 0\nv_maximum3_f32 v13, v13, 0, 0\n"
"v_mfma_f32_16x16x32_bf16 v[20:23], v[164:167], v[136:139], v[20:23]\ns_waitcnt lgkmcnt(2)\nv_maximum3_f32 v14, v14, 0, 0\nv_maximum3_f32 v15, v15, 0, 0\n"
"v_mfma_f32_16x16x32_bf16 v[16:19], v[168:171], v[124:127], v[16:19]\nv_cvt_pk_bf16_f32 v12, v12, v13\nv_cvt_pk_bf16_f32 v13, v14, v15\n"
"v_mfma_f32_16x16x32_bf16 v[20:23], v[168:171], v[140:143], v[20:23]\nds_read_b64_tr_b16 v[176:177], v229 offset:16384\nds_read_b64_tr_b16 v[178:179], v229 offset:18432\n"
"v_mfma_f32_16x16x32_bf16 v[24:27], v[156:159], v[144:147], v[24:27]\nds_read_b64_tr_b16 v[180:181], v229 offset:32768\nds_read_b64_tr_b16 v[182:183], v229 offset:34816\n"
"v_mfma_f32_16x16x32_bf16 v[28:31], v[156:159], v[204:207], v[28:31]\nv_maximum3_f32 v16, v16, 0, 0\nv_maximum3_f32 v17, v17, 0, 0\n"
"v_mfma_f32_16x16x32_bf16 v[24:27], v[160:163], v[148:151], v[24:27]\nv_maximum3_f32 v18, v18, 0, 0\nv_maximum3_f32 v19, v19, 0, 0\n"
"v_mfma_f32_16x16x32_bf16 v[28:31], v[160:163], v[208:211], v[28:31]\nv_cvt_pk_bf16_f32 v2, v16, v17\nv_cvt_pk_bf16_f32 v3, v18, v19\n"
"v_mfma_f32_16x16x32_bf16 v[24:27], v[164:167], v[152:155], v[24:27]\nds_write_b128 v233, v[0:3]\nv_maximum3_f32 v20, v20, 0, 0\nv_maximum3_f32 v21, v21, 0, 0\n"
"v_mfma_f32_16x16x32_bf16 v[28:31], v[164:167], v[212:215], v[28:31]\nv_maximum3_f32 v22, v22, 0, 0\nv_maximum3_f32 v23, v23, 0, 0\n"
"v_mfma_f32_16x16x32_bf16 v[24:27], v[168:171], v[200:203], v[24:27]\nv_cvt_pk_bf16_f32 v6, v20, v21\nv_cvt_pk_bf16_f32 v7, v22, v23\n"
"v_mfma_f32_16x16x32_bf16 v[28:31], v[168:171], v[216:219], v[28:31]\nds_write_b128 v233, v[4:7] offset:1024\nds_read_b64_tr_b16 v[184:185], v229 offset:49152\n"
"ds_read_b64_tr_b16 v[186:187], v229 offset:51200\n"
"v_mfma_f32_16x16x32_bf16 v[32:35], v[188:191], v[112:115], v[32:35]\nds_read_b64_tr_b16 v[156:157], v230\nds_read_b64_tr_b16 v[158:159], v230 offset:2048\n"
"v_mfma_f32_16x16x32_bf16 v[36:39], v[188:191], v[128:131], v[36:39]\nv_maximum3_f32 v24, v24, 0, 0\nv_maximum3_f32 v25, v25, 0, 0\n"
"v_mfma_f32_16x16x32_bf16 v[32:35], v[192:195], v[116:119], v[32:35]\nv_maximum3_f32 v26, v26, 0, 0\nv_maximum3_f32 v27, v27, 0, 0\n"
"v_mfma_f32_16x16x32_bf16 v[36:39], v[192:195], v[132:135], v[36:39]\nv_cvt_pk_bf16_f32 v10, v24, v25\nv_cvt_pk_bf16_f32 v11, v26, v27\n"
"v_mfma_f32_16x16x32_bf16 v[32:35], v[196:199], v[120:123], v[32:35]\nds_write_b128 v233, v[8:11] offset:2048\nv_maximum3_f32 v28, v28, 0, 0\nv_maximum3_f32 v29, v29, 0, 0\n"
"v_mfma_f32_16x16x32_bf16 v[36:39], v[196:199], v[136:139], v[36:39]\nv_maximum3_f32 v30, v30, 0, 0\nv_maximum3_f32 v31, v31, 0, 0\n"
"v_mfma_f32_16x16x32_bf16 v[32:35], v[220:223], v[124:127], v[32:35]\nv_cvt_pk_bf16_f32 v14, v28, v29\nv_cvt_pk_bf16_f32 v15, v30, v31\n"
"v_mfma_f32_16x16x32_bf16 v[36:39], v[220:223], v[140:143], v[36:39]\nds_write_b128 v233, v[12:15] offset:3072\nds_read_b64_tr_b16 v[160:161], v230 offset:16384\n"
"ds_read_b64_tr_b16 v[162:163], v230 offset:18432\n"
"v_mfma_f32_16x16x32_bf16 v[40:43], v[188:191], v[144:147], v[40:43]\nds_read_b64_tr_b16 v[164:165], v230 offset:32768\nds_read_b64_tr_b16 v[166:167], v230 offset:34816\n"
"v_mfma_f32_16x16x32_bf16 v[44:47], v[188:191], v[204:207], v[44:47]\nv_maximum3_f32 v32, v32, 0, 0\nv_maximum3_f32 v33, v33, 0, 0\n"
"v_mfma_f32_16x16x32_bf16 v[40:43], v[192:195], v[148:151], v[40:43]\nv_maximum3_f32 v34, v34, 0, 0\nv_maximum3_f32 v35, v35, 0, 0\ns_waitcnt lgkmcnt(4)\n"
"s_barrier\n"
"v_mfma_f32_16x16x32_bf16 v[44:47], v[192:195], v[208:211], v[44:47]\nv_cvt_pk_bf16_f32 v32, v32, v33\nv_cvt_pk_bf16_f32 v33, v34, v35\n"
"v_mfma_f32_16x16x32_bf16 v[40:43], v[196:199], v[152:155], v[40:43]\nv_maximum3_f32 v36, v36, 0, 0\nv_maximum3_f32 v37, v37, 0, 0\nds_read_b64_tr_b16 v[16:17], v232\n"
"v_mfma_f32_16x16x32_bf16 v[44:47], v[196:199], v[212:215], v[44:47]\nv_maximum3_f32 v38, v38, 0, 0\nv_maximum3_f32 v39, v39, 0, 0\n"
"v_mfma_f32_16x16x32_bf16 v[40:43], v[220:223], v[200:203], v[40:43]\nv_cvt_pk_bf16_f32 v36, v36, v37\nv_cvt_pk_bf16_f32 v37, v38, v39\nds_read_b64_tr_b16 v[18:19], v232 offset:2048\n"
"v_mfma_f32_16x16x32_bf16 v[44:47], v[220:223], v[216:219], v[44:47]\nds_read_b64_tr_b16 v[168:169], v230 offset:49152\nds_read_b64_tr_b16 v[170:171], v230 offset:51200\n"
"v_mfma_f32_16x16x32_bf16 v[48:51], v[172:175], v[112:115], v[48:51]\nds_read_b64_tr_b16 v[0:1], v231\nds_read_b64_tr_b16 v[2:3], v231 offset:2048\n"
"v_mfma_f32_16x16x32_bf16 v[52:55], v[172:175], v[128:131], v[52:55]\nv_maximum3_f32 v40, v40, 0, 0\nv_maximum3_f32 v41, v41, 0, 0\n"
"v_mfma_f32_16x16x32_bf16 v[48:51], v[176:179], v[116:119], v[48:51]\nv_maximum3_f32 v42, v42, 0, 0\nv_maximum3_f32 v43, v43, 0, 0\nds_read_b64_tr_b16 v[20:21], v232 offset:16384\n"
"v_mfma_f32_16x16x32_bf16 v[52:55], v[176:179], v[132:135], v[52:55]\nv_cvt_pk_bf16_f32 v40, v40, v41\nv_cvt_pk_bf16_f32 v41, v42, v43\n"
"v_mfma_f32_16x16x32_bf16 v[48:51], v[180:183], v[120:123], v[48:51]\nv_maximum3_f32 v44, v44, 0, 0\nv_maximum3_f32 v45, v45, 0, 0\nds_read_b64_tr_b16 v[22:23], v232 offset:18432\n"
"v_mfma_f32_16x16x32_bf16 v[52:55], v[180:183], v[136:139], v[52:55]\nv_maximum3_f32 v46, v46, 0, 0\nv_maximum3_f32 v47, v47, 0, 0\n"
"v_mfma_f32_16x16x32_bf16 v[48:51], v[184:187], v[124:127], v[48:51]\nv_cvt_pk_bf16_f32 v44, v44, v45\nv_cvt_pk_bf16_f32 v45, v46, v47\nds_read_b64_tr_b16 v[24:25], v232 offset:32768\n"
"v_mfma_f32_16x16x32_bf16 v[52:55], v[184:187], v[140:143], v[52:55]\nds_read_b64_tr_b16 v[4:5], v231 offset:16384\nds_read_b64_tr_b16 v[6:7], v231 offset:18432\n"
"v_mfma_f32_16x16x32_bf16 v[56:59], v[172:175], v[144:147], v[56:59]\nds_read_b64_tr_b16 v[8:9], v231 offset:32768\nds_read_b64_tr_b16 v[10:11], v231 offset:34816\n"
"v_mfma_f32_16x16x32_bf16 v[60:63], v[172:175], v[204:207], v[60:63]\nv_maximum3_f32 v48, v48, 0, 0\nv_maximum3_f32 v49, v49, 0, 0\n"
"v_mfma_f32_16x16x32_bf16 v[56:59], v[176:179], v[148:151], v[56:59]\nv_maximum3_f32 v50, v50, 0, 0\nv_maximum3_f32 v51, v51, 0, 0\nds_read_b64_tr_b16 v[26:27], v232 offset:34816\n"
"v_mfma_f32_16x16x32_bf16 v[60:63], v[176:179], v[208:211], v[60:63]\nv_cvt_pk_bf16_f32 v34, v48, v49\nv_cvt_pk_bf16_f32 v35, v50, v51\n"
"v_mfma_f32_16x16x32_bf16 v[56:59], v[180:183], v[152:155], v[56:59]\nds_write_b128 v233, v[32:35] offset:16384\nv_maximum3_f32 v52, v52, 0, 0\nv_maximum3_f32 v53, v53, 0, 0\n"
"v_mfma_f32_16x16x32_bf16 v[60:63], v[180:183], v[212:215], v[60:63]\nv_maximum3_f32 v54, v54, 0, 0\nv_maximum3_f32 v55, v55, 0, 0\n"
"v_mfma_f32_16x16x32_bf16 v[56:59], v[184:187], v[200:203], v[56:59]\nv_cvt_pk_bf16_f32 v38, v52, v53\nv_cvt_pk_bf16_f32 v39, v54, v55\n"
"v_mfma_f32_16x16x32_bf16 v[60:63], v[184:187], v[216:219], v[60:63]\ns_waitcnt lgkmcnt(15)\nds_write_b128 v233, v[36:39] offset:17408\nds_read_b64_tr_b16 v[12:13], v231 offset:49152\n"
"ds_read_b64_tr_b16 v[14:15], v231 offset:51200\n"
"v_mfma_f32_16x16x32_bf16 v[64:67], v[156:159], v[112:115], v[64:67]\nds_read_b64_tr_b16 v[28:29], v232 offset:49152\nds_read_b64_tr_b16 v[30:31], v232 offset:51200\n"
"v_mfma_f32_16x16x32_bf16 v[68:71], v[156:159], v[128:131], v[68:71]\nv_maximum3_f32 v56, v56, 0, 0\nv_maximum3_f32 v57, v57, 0, 0\n"
"v_mfma_f32_16x16x32_bf16 v[64:67], v[160:163], v[116:119], v[64:67]\nv_maximum3_f32 v58, v58, 0, 0\nv_maximum3_f32 v59, v59, 0, 0\n"
"v_mfma_f32_16x16x32_bf16 v[68:71], v[160:163], v[132:135], v[68:71]\nv_cvt_pk_bf16_f32 v42, v56, v57\nv_cvt_pk_bf16_f32 v43, v58, v59\n"
"v_mfma_f32_16x16x32_bf16 v[64:67], v[164:167], v[120:123], v[64:67]\nds_write_b128 v233, v[40:43] offset:18432\nv_maximum3_f32 v60, v60, 0, 0\nv_maximum3_f32 v61, v61, 0, 0\n"
"v_mfma_f32_16x16x32_bf16 v[68:71], v[164:167], v[136:139], v[68:71]\ns_waitcnt lgkmcnt(15)\nv_maximum3_f32 v62, v62, 0, 0\nv_maximum3_f32 v63, v63, 0, 0\n"
"v_mfma_f32_16x16x32_bf16 v[64:67], v[168:171], v[124:127], v[64:67]\nv_cvt_pk_bf16_f32 v46, v60, v61\nv_cvt_pk_bf16_f32 v47, v62, v63\n"
"v_mfma_f32_16x16x32_bf16 v[68:71], v[168:171], v[140:143], v[68:71]\nds_write_b128 v233, v[44:47] offset:19456\n"
"v_mfma_f32_16x16x32_bf16 v[72:75], v[156:159], v[144:147], v[72:75]\nds_read_b64_tr_b16 v[188:189], v236\nds_read_b64_tr_b16 v[190:191], v236 offset:4096\n"
"v_mfma_f32_16x16x32_bf16 v[76:79], v[156:159], v[204:207], v[76:79]\nv_maximum3_f32 v64, v64, 0, 0\nv_maximum3_f32 v65, v65, 0, 0\n"
"v_mfma_f32_16x16x32_bf16 v[72:75], v[160:163], v[148:151], v[72:75]\nv_maximum3_f32 v66, v66, 0, 0\nv_maximum3_f32 v67, v67, 0, 0\ns_waitcnt lgkmcnt(2)\n"
"s_barrier\n"
"v_mfma_f32_16x16x32_bf16 v[76:79], v[160:163], v[208:211], v[76:79]\nv_cvt_pk_bf16_f32 v64, v64, v65\nv_cvt_pk_bf16_f32 v65, v66, v67\n"
"v_mfma_f32_16x16x32_bf16 v[72:75], v[164:167], v[152:155], v[72:75]\nv_maximum3_f32 v68, v68, 0, 0\nv_maximum3_f32 v69, v69, 0, 0\n"
"v_mfma_f32_16x16x32_bf16 v[76:79], v[164:167], v[212:215], v[76:79]\nv_maximum3_f32 v70, v70, 0, 0\nv_maximum3_f32 v71, v71, 0, 0\n"
"v_mfma_f32_16x16x32_bf16 v[72:75], v[168:171], v[200:203], v[72:75]\nv_cvt_pk_bf16_f32 v68, v68, v69\nv_cvt_pk_bf16_f32 v69, v70, v71\n"
"v_mfma_f32_16x16x32_bf16 v[76:79], v[168:171], v[216:219], v[76:79]\nds_read_b64_tr_b16 v[192:193], v237\nds_read_b64_tr_b16 v[194:195], v237 offset:4096\n"
"v_mfma_f32_16x16x32_bf16 v[80:83], v[0:3], v[112:115], v[80:83]\nds_read_b64_tr_b16 v[196:197], v238\nds_read_b64_tr_b16 v[198:199], v238 offset:4096\n"
"v_mfma_f32_16x16x32_bf16 v[84:87], v[0:3], v[128:131], v[84:87]\nv_maximum3_f32 v72, v72, 0, 0\nv_maximum3_f32 v73, v73, 0, 0\n"
"v_mfma_f32_16x16x32_bf16 v[80:83], v[4:7], v[116:119], v[80:83]\nv_maximum3_f32 v74, v74, 0, 0\nv_maximum3_f32 v75, v75, 0, 0\n"
"v_mfma_f32_16x16x32_bf16 v[84:87], v[4:7], v[132:135], v[84:87]\nv_cvt_pk_bf16_f32 v72, v72, v73\nv_cvt_pk_bf16_f32 v73, v74, v75\n"
"v_mfma_f32_16x16x32_bf16 v[80:83], v[8:11], v[120:123], v[80:83]\nv_maximum3_f32 v76, v76, 0, 0\nv_maximum3_f32 v77, v77, 0, 0\n"
"v_mfma_f32_16x16x32_bf16 v[84:87], v[8:11], v[136:139], v[84:87]\nv_maximum3_f32 v78, v78, 0, 0\nv_maximum3_f32 v79, v79, 0, 0\n"
"v_mfma_f32_16x16x32_bf16 v[80:83], v[12:15], v[124:127], v[80:83]\nv_cvt_pk_bf16_f32 v76, v76, v77\nv_cvt_pk_bf16_f32 v77, v78, v79\n"
"v_mfma_f32_16x16x32_bf16 v[84:87], v[12:15], v[140:143], v[84:87]\nds_read_b64_tr_b16 v[220:221], v239\nds_read_b64_tr_b16 v[222:223], v239 offset:4096\n"
"v_mfma_f32_16x16x32_bf16 v[88:91], v[0:3], v[144:147], v[88:91]\nds_read_b128 v[156:159], v235\nds_read_b128 v[160:163], v235 offset:1024\n"
"v_mfma_f32_16x16x32_bf16 v[92:95], v[0:3], v[204:207], v[92:95]\nv_maximum3_f32 v80, v80, 0, 0\nv_maximum3_f32 v81, v81, 0, 0\n"
"v_mfma_f32_16x16x32_bf16 v[88:91], v[4:7], v[148:151], v[88:91]\nv_maximum3_f32 v82, v82, 0, 0\nv_maximum3_f32 v83, v83, 0, 0\n"
"v_mfma_f32_16x16x32_bf16 v[92:95], v[4:7], v[208:211], v[92:95]\nv_cvt_pk_bf16_f32 v66, v80, v81\nv_cvt_pk_bf16_f32 v67, v82, v83\n"
"v_mfma_f32_16x16x32_bf16 v[88:91], v[8:11], v[152:155], v[88:91]\nds_write_b128 v233, v[64:67] offset:32768\nv_maximum3_f32 v84, v84, 0, 0\nv_maximum3_f32 v85, v85, 0, 0\n"
"v_mfma_f32_16x16x32_bf16 v[92:95], v[8:11], v[212:215], v[92:95]\nv_maximum3_f32 v86, v86, 0, 0\nv_maximum3_f32 v87, v87, 0, 0\n"
"v_mfma_f32_16x16x32_bf16 v[88:91], v[12:15], v[200:203], v[88:91]\nv_cvt_pk_bf16_f32 v70, v84, v85\nv_cvt_pk_bf16_f32 v71, v86, v87\n"
"v_mfma_f32_16x16x32_bf16 v[92:95], v[12:15], v[216:219], v[92:95]\nds_write_b128 v233, v[68:71] offset:33792\n"
"v_mfma_f32_16x16x32_bf16 v[96:99], v[16:19], v[112:115], v[96:99]\nds_read_b128 v[164:167], v235 offset:2048\nds_read_b128 v[168:171], v235 offset:3072\n"
"v_mfma_f32_16x16x32_bf16 v[100:103], v[16:19], v[128:131], v[100:103]\nv_maximum3_f32 v88, v88, 0, 0\nv_maximum3_f32 v89, v89, 0, 0\n"
"v_mfma_f32_16x16x32_bf16 v[96:99], v[20:23], v[116:119], v[96:99]\nv_maximum3_f32 v90, v90, 0, 0\nv_maximum3_f32 v91, v91, 0, 0\n"
"v_mfma_f32_16x16x32_bf16 v[100:103], v[20:23], v[132:135], v[100:103]\nv_cvt_pk_bf16_f32 v74, v88, v89\nv_cvt_pk_bf16_f32 v75, v90, v91\n"
"v_mfma_f32_16x16x32_bf16 v[96:99], v[24:27], v[120:123], v[96:99]\nds_write_b128 v233, v[72:75] offset:34816\nv_maximum3_f32 v92, v92, 0, 0\nv_maximum3_f32 v93, v93, 0, 0\n"
"v_mfma_f32_16x16x32_bf16 v[100:103], v[24:27], v[136:139], v[100:103]\nv_maximum3_f32 v94, v94, 0, 0\nv_maximum3_f32 v95, v95, 0, 0\n"
"v_mfma_f32_16x16x32_bf16 v[96:99], v[28:31], v[124:127], v[96:99]\nv_cvt_pk_bf16_f32 v78, v92, v93\nv_cvt_pk_bf16_f32 v79, v94, v95\n"
"v_mfma_f32_16x16x32_bf16 v[100:103], v[28:31], v[140:143], v[100:103]\nds_write_b128 v233, v[76:79] offset:35840\n"
"v_mfma_f32_16x16x32_bf16 v[104:107], v[16:19], v[144:147], v[104:107]\ns_nop 1\n"
"v_mfma_f32_16x16x32_bf16 v[108:111], v[16:19], v[204:207], v[108:111]\nv_maximum3_f32 v96, v96, 0, 0\nv_maximum3_f32 v97, v97, 0, 0\n"
"v_mfma_f32_16x16x32_bf16 v[104:107], v[20:23], v[148:151], v[104:107]\nv_maximum3_f32 v98, v98, 0, 0\nv_maximum3_f32 v99, v99, 0, 0\ns_waitcnt lgkmcnt(0)\n"
"s_barrier\n"
"v_mfma_f32_16x16x32_bf16 v[108:111], v[20:23], v[208:211], v[108:111]\nv_cvt_pk_bf16_f32 v96, v96, v97\nv_cvt_pk_bf16_f32 v97, v98, v99\n"
"v_mfma_f32_16x16x32_bf16 v[104:107], v[24:27], v[152:155], v[104:107]\nds_write_b64 v234, v[96:97]\nv_maximum3_f32 v100, v100, 0, 0\nv_maximum3_f32 v101, v101, 0, 0\n"
"v_mfma_f32_16x16x32_bf16 v[108:111], v[24:27], v[212:215], v[108:111]\nv_maximum3_f32 v102, v102, 0, 0\nv_maximum3_f32 v103, v103, 0, 0\n"
"v_mfma_f32_16x16x32_bf16 v[104:107], v[28:31], v[200:203], v[104:107]\nv_cvt_pk_bf16_f32 v100, v100, v101\nv_cvt_pk_bf16_f32 v101, v102, v103\n"
"v_mfma_f32_16x16x32_bf16 v[108:111], v[28:31], v[216:219], v[108:111]\nds_write_b64 v234, v[100:101] offset:1024\n"
"v_mfma_f32_16x16x32_bf16 v[0:3], v[188:191], v[156:159], 0\nds_read_b128 v[112:115], v235 offset:8192\nds_read_b64_tr_b16 v[128:129], v236 offset:8192\n"
"v_mfma_f32_16x16x32_bf16 v[16:19], v[188:191], v[160:163], 0\nv_maximum3_f32 v104, v104, 0, 0\nv_maximum3_f32 v105, v105, 0, 0\n"
"v_mfma_f32_16x16x32_bf16 v[32:35], v[188:191], v[164:167], 0\nv_maximum3_f32 v106, v106, 0, 0\nv_maximum3_f32 v107, v107, 0, 0\n"
"v_mfma_f32_16x16x32_bf16 v[48:51], v[188:191], v[168:171], 0\nv_cvt_pk_bf16_f32 v104, v104, v105\nv_cvt_pk_bf16_f32 v105, v106, v107\n"
"v_mfma_f32_16x16x32_bf16 v[4:7], v[192:195], v[156:159], 0\nds_write_b64 v234, v[104:105] offset:2048\nv_maximum3_f32 v108, v108, 0, 0\nv_maximum3_f32 v109, v109, 0, 0\n"
"v_mfma_f32_16x16x32_bf16 v[20:23], v[192:195], v[160:163], 0\nv_maximum3_f32 v110, v110, 0, 0\nv_maximum3_f32 v111, v111, 0, 0\n"
"v_mfma_f32_16x16x32_bf16 v[36:39], v[192:195], v[164:167], 0\nv_cvt_pk_bf16_f32 v108, v108, v109\nv_cvt_pk_bf16_f32 v109, v110, v111\n"
"v_mfma_f32_16x16x32_bf16 v[52:55], v[192:195], v[168:171], 0\nds_write_b64 v234, v[108:109] offset:3072\nds_read_b64_tr_b16 v[130:131], v236 offset:12288\n"
"v_mfma_f32_16x16x32_bf16 v[8:11], v[196:199], v[156:159], 0\nds_read_b128 v[116:119], v235 offset:9216\nds_read_b128 v[120:123], v235 offset:10240\n"
"v_mfma_f32_16x16x32_bf16 v[24:27], v[196:199], v[160:163], 0\nds_read_b128 v[124:127], v235 offset:11264\nds_read_b64_tr_b16 v[132:133], v237 offset:8192\n"
"v_mfma_f32_16x16x32_bf16 v[40:43], v[196:199], v[164:167], 0\nds_read_b64_tr_b16 v[134:135], v237 offset:12288\nds_read_b64_tr_b16 v[136:137], v238 offset:8192\n"
"v_mfma_f32_16x16x32_bf16 v[56:59], v[196:199], v[168:171], 0\nds_read_b64_tr_b16 v[138:139], v238 offset:12288\nds_read_b64_tr_b16 v[140:141], v239 offset:8192\n"
"v_mfma_f32_16x16x32_bf16 v[12:15], v[220:223], v[156:159], 0\nds_read_b64_tr_b16 v[142:143], v239 offset:12288\n"
"v_mfma_f32_16x16x32_bf16 v[28:31], v[220:223], v[160:163], 0\n"
"v_mfma_f32_16x16x32_bf16 v[44:47], v[220:223], v[164:167], 0\n"
"v_mfma_f32_16x16x32_bf16 v[60:63], v[220:223], v[168:171], 0\ns_waitcnt lgkmcnt(6)\n"
"v_mfma_f32_16x16x32_bf16 v[0:3], v[128:131], v[112:115], v[0:3]\nds_read_b128 v[156:159], v235 offset:16384\n"
"v_mfma_f32_16x16x32_bf16 v[16:19], v[128:131], v[116:119], v[16:19]\nds_read_b64_tr_b16 v[188:189], v236 offset:16384\n"
"v_mfma_f32_16x16x32_bf16 v[32:35], v[128:131], v[120:123], v[32:35]\nds_read_b64_tr_b16 v[190:191], v236 offset:20480\n"
"v_mfma_f32_16x16x32_bf16 v[48:51], v[128:131], v[124:127], v[48:51]\ns_waitcnt lgkmcnt(5)\nds_read_b128 v[160:163], v235 offset:17408\n"
"v_mfma_f32_16x16x32_bf16 v[4:7], v[132:135], v[112:115], v[4:7]\nds_read_b128 v[164:167], v235 offset:18432\n"
"v_mfma_f32_16x16x32_bf16 v[20:23], v[132:135], v[116:119], v[20:23]\nds_read_b128 v[168:171], v235 offset:19456\n"
"v_mfma_f32_16x16x32_bf16 v[36:39], v[132:135], v[120:123], v[36:39]\nds_read_b64_tr_b16 v[192:193], v237 offset:16384\n"
"v_mfma_f32_16x16x32_bf16 v[52:55], v[132:135], v[124:127], v[52:55]\nds_read_b64_tr_b16 v[194:195], v237 offset:20480\n"
"v_mfma_f32_16x16x32_bf16 v[8:11], v[136:139], v[112:115], v[8:11]\nds_read_b64_tr_b16 v[196:197], v238 offset:16384\n"
"v_mfma_f32_16x16x32_bf16 v[24:27], v[136:139], v[116:119], v[24:27]\nds_read_b64_tr_b16 v[198:199], v238 offset:20480\n"
"v_mfma_f32_16x16x32_bf16 v[40:43], v[136:139], v[120:123], v[40:43]\nds_read_b64_tr_b16 v[220:221], v239 offset:16384\n"
"v_mfma_f32_16x16x32_bf16 v[56:59], v[136:139], v[124:127], v[56:59]\ns_waitcnt lgkmcnt(11)\nds_read_b64_tr_b16 v[222:223], v239 offset:20480\n"
"v_mfma_f32_16x16x32_bf16 v[12:15], v[140:143], v[112:115], v[12:15]\n"
"v_mfma_f32_16x16x32_bf16 v[28:31], v[140:143], v[116:119], v[28:31]\n"
"v_mfma_f32_16x16x32_bf16 v[44:47], v[140:143], v[120:123], v[44:47]\n"
"v_mfma_f32_16x16x32_bf16 v[60:63], v[140:143], v[124:127], v[60:63]\ns_waitcnt lgkmcnt(4)\n"
"v_mfma_f32_16x16x32_bf16 v[0:3], v[188:191], v[156:159], v[0:3]\nds_read_b128 v[112:115], v235 offset:24576\n"
"v_mfma_f32_16x16x32_bf16 v[16:19], v[188:191], v[160:163], v[16:19]\nds_read_b64_tr_b16 v[128:129], v236 offset:24576\n"
"v_mfma_f32_16x16x32_bf16 v[32:35], v[188:191], v[164:167], v[32:35]\nds_read_b64_tr_b16 v[130:131], v236 offset:28672\n"
"v_mfma_f32_16x16x32_bf16 v[48:51], v[188:191], v[168:171], v[48:51]\nds_read_b128 v[116:119], v235 offset:25600\n"
"v_mfma_f32_16x16x32_bf16 v[4:7], v[192:195], v[156:159], v[4:7]\nds_read_b128 v[120:123], v235 offset:26624\n"
"v_mfma_f32_16x16x32_bf16 v[20:23], v[192:195], v[160:163], v[20:23]\nds_read_b128 v[124:127], v235 offset:27648\n"
"v_mfma_f32_16x16x32_bf16 v[36:39], v[192:195], v[164:167], v[36:39]\nds_read_b64_tr_b16 v[132:133], v237 offset:24576\n"
"v_mfma_f32_16x16x32_bf16 v[52:55], v[192:195], v[168:171], v[52:55]\ns_waitcnt lgkmcnt(7)\nds_read_b64_tr_b16 v[134:135], v237 offset:28672\n"
"v_mfma_f32_16x16x32_bf16 v[8:11], v[196:199], v[156:159], v[8:11]\nds_read_b64_tr_b16 v[136:137], v238 offset:24576\n"
"v_mfma_f32_16x16x32_bf16 v[24:27], v[196:199], v[160:163], v[24:27]\nds_read_b64_tr_b16 v[138:139], v238 offset:28672\n"
"v_mfma_f32_16x16x32_bf16 v[40:43], v[196:199], v[164:167], v[40:43]\nds_read_b64_tr_b16 v[140:141], v239 offset:24576\n"
"v_mfma_f32_16x16x32_bf16 v[56:59], v[196:199], v[168:171], v[56:59]\nds_read_b64_tr_b16 v[142:143], v239 offset:28672\n"
"v_mfma_f32_16x16x32_bf16 v[12:15], v[220:223], v[156:159], v[12:15]\nv_add_u32 v236, 0xffff4000, v236\n"
"v_mfma_f32_16x16x32_bf16 v[28:31], v[220:223], v[160:163], v[28:31]\nv_add_u32 v237, 0xffff4000, v237\n"
"v_mfma_f32_16x16x32_bf16 v[44:47], v[220:223], v[164:167], v[44:47]\nv_add_u32 v238, 0xffff4000, v238\n"
"v_mfma_f32_16x16x32_bf16 v[60:63], v[220:223], v[168:171], v[60:63]\nv_add_u32 v239, 0xffff4000, v239\ns_waitcnt lgkmcnt(6)\n"
"v_mfma_f32_16x16x32_bf16 v[0:3], v[128:131], v[112:115], v[0:3]\n"
"v_mfma_f32_16x16x32_bf16 v[16:19], v[128:131], v[116:119], v[16:19]\n"
"v_mfma_f32_16x16x32_bf16 v[32:35], v[128:131], v[120:123], v[32:35]\n"
"v_mfma_f32_16x16x32_bf16 v[48:51], v[128:131], v[124:127], v[48:51]\ns_waitcnt lgkmcnt(0)\ns_barrier\n"
"v_mfma_f32_16x16x32_bf16 v[4:7], v[132:135], v[112:115], v[4:7]\nds_read_b128 v[156:159], v235 offset:32768\n"
"v_mfma_f32_16x16x32_bf16 v[20:23], v[132:135], v[116:119], v[20:23]\nds_read_b64_tr_b16 v[188:189], v236\n"
"v_mfma_f32_16x16x32_bf16 v[36:39], v[132:135], v[120:123], v[36:39]\nds_read_b64_tr_b16 v[190:191], v236 offset:4096\n"
"v_mfma_f32_16x16x32_bf16 v[52:55], v[132:135], v[124:127], v[52:55]\nds_read_b128 v[160:163], v235 offset:33792\n"
"v_mfma_f32_16x16x32_bf16 v[8:11], v[136:139], v[112:115], v[8:11]\nds_read_b128 v[164:167], v235 offset:34816\n"
"v_mfma_f32_16x16x32_bf16 v[24:27], v[136:139], v[116:119], v[24:27]\nds_read_b128 v[168:171], v235 offset:35840\n"
"v_mfma_f32_16x16x32_bf16 v[40:43], v[136:139], v[120:123], v[40:43]\nds_read_b64_tr_b16 v[192:193], v237\n"
"v_mfma_f32_16x16x32_bf16 v[56:59], v[136:139], v[124:127], v[56:59]\nds_read_b64_tr_b16 v[194:195], v237 offset:4096\n"
"v_mfma_f32_16x16x32_bf16 v[12:15], v[140:143], v[112:115], v[12:15]\nds_read_b64_tr_b16 v[196:197], v238\n"
"v_mfma_f32_16x16x32_bf16 v[28:31], v[140:143], v[116:119], v[28:31]\nds_read_b64_tr_b16 v[198:199], v238 offset:4096\n"
"v_mfma_f32_16x16x32_bf16 v[44:47], v[140:143], v[120:123], v[44:47]\nds_read_b64_tr_b16 v[220:221], v239\n"
"v_mfma_f32_16x16x32_bf16 v[60:63], v[140:143], v[124:127], v[60:63]\nds_read_b64_tr_b16 v[222:223], v239 offset:4096\ns_waitcnt lgkmcnt(6)\n"
"v_mfma_f32_16x16x32_bf16 v[0:3], v[188:191], v[156:159], v[0:3]\nds_read_b128 v[112:115], v235 offset:40960\n"
"v_mfma_f32_16x16x32_bf16 v[16:19], v[188:191], v[160:163], v[16:19]\nds_read_b64_tr_b16 v[128:129], v236 offset:8192\n"
"v_mfma_f32_16x16x32_bf16 v[32:35], v[188:191], v[164:167], v[32:35]\nds_read_b64_tr_b16 v[130:131], v236 offset:12288\n"
"v_mfma_f32_16x16x32_bf16 v[48:51], v[188:191], v[168:171], v[48:51]\ns_waitcnt lgkmcnt(5)\nds_read_b128 v[116:119], v235 offset:41984\n"
"v_mfma_f32_16x16x32_bf16 v[4:7], v[192:195], v[156:159], v[4:7]\nds_read_b128 v[120:123], v235 offset:43008\n"
"v_mfma_f32_16x16x32_bf16 v[20:23], v[192:195], v[160:163], v[20:23]\nds_read_b128 v[124:127], v235 offset:44032\n"
"v_mfma_f32_16x16x32_bf16 v[36:39], v[192:195], v[164:167], v[36:39]\nds_read_b64_tr_b16 v[132:133], v237 offset:8192\n"
"v_mfma_f32_16x16x32_bf16 v[52:55], v[192:195], v[168:171], v[52:55]\nds_read_b64_tr_b16 v[134:135], v237 offset:12288\n"
"v_mfma_f32_16x16x32_bf16 v[8:11], v[196:199], v[156:159], v[8:11]\nds_read_b64_tr_b16 v[136:137], v238 offset:8192\n"
"v_mfma_f32_16x16x32_bf16 v[24:27], v[196:199], v[160:163], v[24:27]\nds_read_b64_tr_b16 v[138:139], v238 offset:12288\n"
"v_mfma_f32_16x16x32_bf16 v[40:43], v[196:199], v[164:167], v[40:43]\nds_read_b64_tr_b16 v[140:141], v239 offset:8192\n"
"v_mfma_f32_16x16x32_bf16 v[56:59], v[196:199], v[168:171], v[56:59]\ns_waitcnt lgkmcnt(11)\nds_read_b64_tr_b16 v[142:143], v239 offset:12288\n"
"v_mfma_f32_16x16x32_bf16 v[12:15], v[220:223], v[156:159], v[12:15]\n"
"v_mfma_f32_16x16x32_bf16 v[28:31], v[220:223], v[160:163], v[28:31]\n"
"v_mfma_f32_16x16x32_bf16 v[44:47], v[220:223], v[164:167], v[44:47]\n"
"v_mfma_f32_16x16x32_bf16 v[60:63], v[220:223], v[168:171], v[60:63]\ns_waitcnt lgkmcnt(4)\n"
"v_mfma_f32_16x16x32_bf16 v[0:3], v[128:131], v[112:115], v[0:3]\nds_read_b128 v[156:159], v235 offset:49152\n"
"v_mfma_f32_16x16x32_bf16 v[16:19], v[128:131], v[116:119], v[16:19]\nds_read_b64_tr_b16 v[188:189], v236 offset:16384\n"
"v_mfma_f32_16x16x32_bf16 v[32:35], v[128:131], v[120:123], v[32:35]\nds_read_b64_tr_b16 v[190:191], v236 offset:20480\n"
"v_mfma_f32_16x16x32_bf16 v[48:51], v[128:131], v[124:127], v[48:51]\nds_read_b128 v[160:163], v235 offset:50176\n"
"v_mfma_f32_16x16x32_bf16 v[4:7], v[132:135], v[112:115], v[4:7]\nds_read_b128 v[164:167], v235 offset:51200\n"
"v_mfma_f32_16x16x32_bf16 v[20:23], v[132:135], v[116:119], v[20:23]\nds_read_b128 v[168:171], v235 offset:52224\n"
"v_mfma_f32_16x16x32_bf16 v[36:39], v[132:135], v[120:123], v[36:39]\nds_read_b64_tr_b16 v[192:193], v237 offset:16384\n"
"v_mfma_f32_16x16x32_bf16 v[52:55], v[132:135], v[124:127], v[52:55]\ns_waitcnt lgkmcnt(7)\nds_read_b64_tr_b16 v[194:195], v237 offset:20480\n"
"v_mfma_f32_16x16x32_bf16 v[8:11], v[136:139], v[112:115], v[8:11]\nds_read_b64_tr_b16 v[196:197], v238 offset:16384\n"
"v_mfma_f32_16x16x32_bf16 v[24:27], v[136:139], v[116:119], v[24:27]\nds_read_b64_tr_b16 v[198:199], v238 offset:20480\n"
"v_mfma_f32_16x16x32_bf16 v[40:43], v[136:139], v[120:123], v[40:43]\nds_read_b64_tr_b16 v[220:221], v239 offset:16384\n"
"v_mfma_f32_16x16x32_bf16 v[56:59], v[136:139], v[124:127], v[56:59]\nds_read_b64_tr_b16 v[222:223], v239 offset:20480\n"
"v_mfma_f32_16x16x32_bf16 v[12:15], v[140:143], v[112:115], v[12:15]\n"
"v_mfma_f32_16x16x32_bf16 v[28:31], v[140:143], v[116:119], v[28:31]\ns_mov_b32 %[cnt], 0\n"
"v_mfma_f32_16x16x32_bf16 v[44:47], v[140:143], v[120:123], v[44:47]\ns_mov_b32 %[ps], 0x4000\n"
"v_mfma_f32_16x16x32_bf16 v[60:63], v[140:143], v[124:127], v[60:63]\ns_waitcnt lgkmcnt(6)\n"
"v_mfma_f32_16x16x32_bf16 v[0:3], v[188:191], v[156:159], v[0:3]\n"
"v_mfma_f32_16x16x32_bf16 v[16:19], v[188:191], v[160:163], v[16:19]\n"
"v_mfma_f32_16x16x32_bf16 v[32:35], v[188:191], v[164:167], v[32:35]\n"
"v_mfma_f32_16x16x32_bf16 v[48:51], v[188:191], v[168:171], v[48:51]\ns_waitcnt lgkmcnt(0)\ns_barrier\n"
"v_mfma_f32_16x16x32_bf16 v[4:7], v[192:195], v[156:159], v[4:7]\nds_read_b128 v[112:115], v235 offset:57344\n"
"v_mfma_f32_16x16x32_bf16 v[20:23], v[192:195], v[160:163], v[20:23]\nds_read_b64_tr_b16 v[128:129], v236 offset:24576\n"
"v_mfma_f32_16x16x32_bf16 v[36:39], v[192:195], v[164:167], v[36:39]\nds_read_b64_tr_b16 v[130:131], v236 offset:28672\n"
"v_mfma_f32_16x16x32_bf16 v[52:55], v[192:195], v[168:171], v[52:55]\nds_read_b128 v[116:119], v235 offset:58368\nv_add_u32 v236, 0x8000, v236\n"
"v_mfma_f32_16x16x32_bf16 v[8:11], v[196:199], v[156:159], v[8:11]\nds_read_b128 v[120:123], v235 offset:59392\n"
"v_mfma_f32_16x16x32_bf16 v[24:27], v[196:199], v[160:163], v[24:27]\nds_read_b128 v[124:127], v235 offset:60416\n"
"v_mfma_f32_16x16x32_bf16 v[40:43], v[196:199], v[164:167], v[40:43]\nds_read_b64_tr_b16 v[132:133], v237 offset:24576\nv_add_u32 v235, 0x10000, v235\n"
"v_mfma_f32_16x16x32_bf16 v[56:59], v[196:199], v[168:171], v[56:59]\nds_read_b64_tr_b16 v[134:135], v237 offset:28672\n"
"v_mfma_f32_16x16x32_bf16 v[12:15], v[220:223], v[156:159], v[12:15]\nds_read_b64_tr_b16 v[136:137], v238 offset:24576\nv_add_u32 v237, 0x8000, v237\n"
"v_mfma_f32_16x16x32_bf16 v[28:31], v[220:223], v[160:163], v[28:31]\nds_read_b64_tr_b16 v[138:139], v238 offset:28672\n"
"v_mfma_f32_16x16x32_bf16 v[44:47], v[220:223], v[164:167], v[44:47]\nds_read_b64_tr_b16 v[140:141], v239 offset:24576\nv_add_u32 v238, 0x8000, v238\n"
"v_mfma_f32_16x16x32_bf16 v[60:63], v[220:223], v[168:171], v[60:63]\nds_read_b64_tr_b16 v[142:143], v239 offset:28672\nv_add_u32 v239, 0x8000, v239\nL3_%=:\n"
"s_waitcnt lgkmcnt(6)\n"
"v_mfma_f32_16x16x32_bf16 v[0:3], v[128:131], v[112:115], v[0:3]\ns_bitcmp1_b32 0x108421, %[cnt]\ns_cselect_b32 %[dl], 0xffff0000, %[ps]\n"
"v_mfma_f32_16x16x32_bf16 v[16:19], v[128:131], v[116:119], v[16:19]\ns_bitcmp1_b32 0x421084, %[cnt]\ns_cselect_b32 %[xs], 0xffff0000, %[ps]\n"
"v_mfma_f32_16x16x32_bf16 v[32:35], v[128:131], v[120:123], v[32:35]\ns_add_u32 %[cnt], %[cnt], 1\ns_cmp_eq_u32 %[cnt], 24\n"
"v_mfma_f32_16x16x32_bf16 v[48:51], v[128:131], v[124:127], v[48:51]\ns_waitcnt lgkmcnt(0)\ns_barrier\n"
"v_mfma_f32_16x16x32_bf16 v[4:7], v[132:135], v[112:115], v[4:7]\nds_read_b128 v[156:159], v235\n"
"v_mfma_f32_16x16x32_bf16 v[20:23], v[132:135], v[116:119], v[20:23]\nds_read_b64_tr_b16 v[188:189], v236\n"
"v_mfma_f32_16x16x32_bf16 v[36:39], v[132:135], v[120:123], v[36:39]\nds_read_b64_tr_b16 v[190:191], v236 offset:4096\n"
"v_mfma_f32_16x16x32_bf16 v[52:55], v[132:135], v[124:127], v[52:55]\nds_read_b128 v[160:163], v235 offset:1024\n"
"v_mfma_f32_16x16x32_bf16 v[8:11], v[136:139], v[112:115], v[8:11]\nds_read_b128 v[164:167], v235 offset:2048\n"
"v_mfma_f32_16x16x32_bf16 v[24:27], v[136:139], v[116:119], v[24:27]\nds_read_b128 v[168:171], v235 offset:3072\n"
"v_mfma_f32_16x16x32_bf16 v[40:43], v[136:139], v[120:123], v[40:43]\nds_read_b64_tr_b16 v[192:193], v237\n"
"v_mfma_f32_16x16x32_bf16 v[56:59], v[136:139], v[124:127], v[56:59]\nds_read_b64_tr_b16 v[194:195], v237 offset:4096\n"
"v_mfma_f32_16x16x32_bf16 v[12:15], v[140:143], v[112:115], v[12:15]\nds_read_b64_tr_b16 v[196:197], v238\n"
"v_mfma_f32_16x16x32_bf16 v[28:31], v[140:143], v[116:119], v[28:31]\nds_read_b64_tr_b16 v[198:199], v238 offset:4096\n"
"v_mfma_f32_16x16x32_bf16 v[44:47], v[140:143], v[120:123], v[44:47]\nds_read_b64_tr_b16 v[220:221], v239\n"
"v_mfma_f32_16x16x32_bf16 v[60:63], v[140:143], v[124:127], v[60:63]\nds_read_b64_tr_b16 v[222:223], v239 offset:4096\ns_waitcnt lgkmcnt(6)\n"
"v_mfma_f32_16x16x32_bf16 v[0:3], v[188:191], v[156:159], v[0:3]\nds_read_b128 v[112:115], v235 offset:8192\n"
"v_mfma_f32_16x16x32_bf16 v[16:19], v[188:191], v[160:163], v[16:19]\nds_read_b64_tr_b16 v[128:129], v236 offset:8192\n"
"v_mfma_f32_16x16x32_bf16 v[32:35], v[188:191], v[164:167], v[32:35]\nds_read_b64_tr_b16 v[130:131], v236 offset:12288\n"
"v_mfma_f32_16x16x32_bf16 v[48:51], v[188:191], v[168:171], v[48:51]\ns_waitcnt lgkmcnt(5)\nds_read_b128 v[116:119], v235 offset:9216\n"
"v_mfma_f32_16x16x32_bf16 v[4:7], v[192:195], v[156:159], v[4:7]\nds_read_b128 v[120:123], v235 offset:10240\n"
"v_mfma_f32_16x16x32_bf16 v[20:23], v[192:195], v[160:163], v[20:23]\nds_read_b128 v[124:127], v235 offset:11264\n"
"v_mfma_f32_16x16x32_bf16 v[36:39], v[192:195], v[164:167], v[36:39]\nds_read_b64_tr_b16 v[132:133], v237 offset:8192\n"
"v_mfma_f32_16x16x32_bf16 v[52:55], v[192:195], v[168:171], v[52:55]\nds_read_b64_tr_b16 v[134:135], v237 offset:12288\n"
"v_mfma_f32_16x16x32_bf16 v[8:11], v[196:199], v[156:159], v[8:11]\nds_read_b64_tr_b16 v[136:137], v238 offset:8192\n"
"v_mfma_f32_16x16x32_bf16 v[24:27], v[196:199], v[160:163], v[24:27]\nds_read_b64_tr_b16 v[138:139], v238 offset:12288\n"
"v_mfma_f32_16x16x32_bf16 v[40:43], v[196:199], v[164:167], v[40:43]\nds_read_b64_tr_b16 v[140:141], v239 offset:8192\n"
"v_mfma_f32_16x16x32_bf16 v[56:59], v[196:199], v[168:171], v[56:59]\ns_waitcnt lgkmcnt(11)\nds_read_b64_tr_b16 v[142:143], v239 offset:12288\n"
"v_mfma_f32_16x16x32_bf16 v[12:15], v[220:223], v[156:159], v[12:15]\nv_add_u32 v235, %[dl], v235\nv_add_u32 v236, %[xs], v236\n"
"v_mfma_f32_16x16x32_bf16 v[28:31], v[220:223], v[160:163], v[28:31]\nv_add_u32 v237, %[xs], v237\n"
"v_mfma_f32_16x16x32_bf16 v[44:47], v[220:223], v[164:167], v[44:47]\nv_add_u32 v238, %[xs], v238\n"
"v_mfma_f32_16x16x32_bf16 v[60:63], v[220:223], v[168:171], v[60:63]\nv_add_u32 v239, %[xs], v239\ns_cbranch_scc0 L3_%=\ns_waitcnt lgkmcnt(4)\n"
"v_mfma_f32_16x16x32_bf16 v[0:3], v[128:131], v[112:115], v[0:3]\n"
"v_mfma_f32_16x16x32_bf16 v[16:19], v[128:131], v[116:119], v[16:19]\n"
"v_mfma_f32_16x16x32_bf16 v[32:35], v[128:131], v[120:123], v[32:35]\n"
"v_mfma_f32_16x16x32_bf16 v[48:51], v[128:131], v[124:127], v[48:51]\n"
"v_mfma_f32_16x16x32_bf16 v[4:7], v[132:135], v[112:115], v[4:7]\n"
"v_mfma_f32_16x16x32_bf16 v[20:23], v[132:135], v[116:119], v[20:23]\n"
"v_mfma_f32_16x16x32_bf16 v[36:39], v[132:135], v[120:123], v[36:39]\n"
"v_mfma_f32_16x16x32_bf16 v[52:55], v[132:135], v[124:127], v[52:55]\ns_waitcnt lgkmcnt(0)\n"
"v_mfma_f32_16x16x32_bf16 v[8:11], v[136:139], v[112:115], v[8:11]\n"
"v_mfma_f32_16x16x32_bf16 v[24:27], v[136:139], v[116:119], v[24:27]\n"
"v_mfma_f32_16x16x32_bf16 v[40:43], v[136:139], v[120:123], v[40:43]\n"
"v_mfma_f32_16x16x32_bf16 v[56:59], v[136:139], v[124:127], v[56:59]\n"
"v_mfma_f32_16x16x32_bf16 v[12:15], v[140:143], v[112:115], v[12:15]\n"
"v_mfma_f32_16x16x32_bf16 v[28:31], v[140:143], v[116:119], v[28:31]\n"
"v_mfma_f32_16x16x32_bf16 v[44:47], v[140:143], v[120:123], v[44:47]\n"
"v_mfma_f32_16x16x32_bf16 v[60:63], v[140:143], v[124:127], v[60:63]\ns_nop 7\n"
        "s_waitcnt vmcnt(0)\n"
        : "={v[0:31]}"(accA), "={v[32:63]}"(accB), "+{v224}"(ax0), "+{v225}"(ax1), "+{v226}"(aw[0]), "+{v227}"(aw[1]), "+{v228}"(aw[2]),
          "+{v229}"(aw[3]), "+{v230}"(aw[4]), "+{v231}"(aw[5]), "+{v232}"(aw[6]), "+{v235}"(ah), "+{v236}"(a2[0]), "+{v237}"(a2[1]),
          "+{v238}"(a2[2]), "+{v239}"(a2[3]), "+{v248}"(cpf), "+{v249}"(tch), [cnt] "=&s"(cnt), [dl] "=&s"(dl), [ps] "=&s"(ps), [xs] "=&s"(xs)
        : "{v233}"(hw), "{v234}"(hw6)
        : "memory", "scc", "v64", "v65", "v66", "v67", "v68", "v69", "v70", "v71", "v72", "v73", "v74", "v75", "v76", "v77", "v78", "v79", "v80", "v81", "v82", "v83", "v84", "v85", "v86", "v87", "v88", "v89", "v90", "v91", "v92", "v93", "v94", "v95", "v96", "v97", "v98", "v99", "v100", "v101", "v102", "v103", "v104", "v105", "v106", "v107", "v108", "v109", "v110", "v111", "v112", "v113", "v114", "v115", "v116", "v117", "v118", "v119", "v120", "v121", "v122", "v123", "v124", "v125", "v126", "v127", "v128", "v129", "v130", "v131", "v132", "v133", "v134", "v135", "v136", "v137", "v138", "v139", "v140", "v141", "v142", "v143", "v144", "v145", "v146", "v147", "v148", "v149", "v150", "v151", "v152", "v153", "v154", "v155", "v156", "v157", "v158", "v159", "v160", "v161", "v162", "v163", "v164", "v165", "v166", "v167", "v168", "v169", "v170", "v171", "v172", "v173", "v174", "v175", "v176", "v177", "v178", "v179", "v180", "v181", "v182", "v183", "v184", "v185", "v186", "v187", "v188", "v189", "v190", "v191", "v192", "v193", "v194", "v195", "v196", "v197", "v198", "v199", "v200", "v201", "v202", "v203", "v204", "v205", "v206", "v207", "v208", "v209", "v210", "v211", "v212", "v213", "v214", "v215", "v216", "v217", "v218", "v219", "v220", "v221", "v222", "v223");
  }
  f32x4 acc[4][4];
#pragma unroll
  for (int n = 0; n < 4; ++n)
#pragma unroll
    for (int e = 0; e < 4; ++e) {
      acc[0][n][e] = accA[n * 4 + e]; acc[1][n][e] = accA[16 + n * 4 + e];
      acc[2][n][e] = accB[n * 4 + e]; acc[3][n][e] = accB[16 + n * 4 + e];
    }
  STAMP(4);
  {
    unsigned lane;
    asm volatile("v_mbcnt_lo_u32_b32 %0, -1, 0\n\tv_mbcnt_hi_u32_b32 %0, -1, %0" : "=v"(lane));
    const __amdgpu_buffer_rsrc_t rx = __builtin_amdgcn_make_buffer_rsrc(ws + kWsX, 0, kXchBytes, 0x00020000);
    auto part = [&](auto kh_) {
    constexpr bool kh = decltype(kh_)::value;
    u32x4 sv[4] = {};
    asm volatile("" ::: "memory");
#pragma unroll
    for (int e = 0; e < 2; ++e)
#pragma unroll
      for (int n = 0; n < 4; ++n) {
        if (XBF) {
          if (n & 1) continue;
          const f32x4 s0 = kh ? acc[e][n] : acc[2 + e][n], s1 = kh ? acc[e][n + 1] : acc[2 + e][n + 1];
          u32x4& snd = sv[e * 2 + n / 2];
          snd = (u32x4){pack_bf16(s0[0], s0[1]), pack_bf16(s0[2], s0[3]), pack_bf16(s1[0], s1[1]), pack_bf16(s1[2], s1[3])};
          st128<16>(snd, rx, lane * 16 + (e * 2 + n / 2) * 1024, (wg ^ 8) * 32768 + wave * 8192);
        } else {
          u32x4 snd = __builtin_bit_cast(u32x4, kh ? acc[e][n] : acc[2 + e][n]);
          st128<16>(snd, rx, lane * 16 + n * 1024, (wg ^ 8) * 32768 + wave * 8192 + e * 4096);
        }
      }
#pragma unroll
    for (int e = 0; e < 2; ++e)
#pragma unroll
      for (int n = 0; n < 4; ++n) *(LAS f32x4*)(smem + 98304 + wave * 8192 + e * 4096 + n * 1024 + lane * 16) = kh ? acc[2 + e][n] : acc[e][n];
    asm volatile("" ::"v"(sv[0]), "v"(sv[1]), "v"(sv[2]), "v"(sv[3]) : "memory");
    };
    if (bj >> 3) part(std::true_type{}); else part(std::false_type{});
    if (!TL2) { wait_lgkm0(); bar(); }
    STAMP(5);
    wait_vm<0>();
    st_flag<1>((u64*)(ws + kWsXF + (wg ^ 8) * 512 + wave * XST), gen + 1);
    STAMP(6);
    if (TL2) { wait_lgkm0(); bar(); }
    STAMP_FLUSH();
    asm volatile("LCODE_E2:");
  }
}

}

extern "C" void kernel_launch(void* const* d_in, const int* in_sizes, int n_in, void* d_out, int out_size, void* d_ws,
                              size_t ws_size, hipStream_t stream) {
  (void)in_sizes; (void)n_in; (void)out_size; (void)ws_size;
  mlp_fused<<<256, 512, 0, stream>>>((const char*)d_in[0], (const char*)d_in[1], (const char*)d_in[2], (char*)d_out, (char*)d_ws);
}
```
